```python
import torch

def _softmax(x, axis=-1):
    return torch.softmax(x, dim=axis)
def _gelu(x):
    return torch.nn.functional.gelu(x, approximate="tanh")
def _silu(x):
    return torch.nn.functional.silu(x)
def _sigmoid(x):
    return torch.sigmoid(x)
def _layernorm(x, axis=-1, eps=1e-5):
    m = x.mean(dim=axis, keepdim=True)
    v = x.var(dim=axis, keepdim=True, unbiased=False)
    return (x - m) / torch.sqrt(v + eps)
def _scatter_add(tgt, idx, src, axis=0):
    return tgt.clone().index_add_(axis, idx, src)
def _segment_sum(src, seg, n):
    out = torch.zeros((n,) + src.shape[1:], dtype=src.dtype)
    return out.index_add_(0, seg, src)
def _pad(x, pw):
    flat = tuple(v for pair in reversed(pw) for v in pair)
    return torch.nn.functional.pad(x, flat)

kernel_name = 'interleave_layers__ssm_conv_h3__linear_kernel_hedgehog__prompt'

def setup_inputs():

    _w = (lambda a: (lambda t: t.to(torch.bfloat16) if t.is_floating_point() else t)(torch.as_tensor(a)))
    def _A_setup():
        B, S, D = 1, 2048, 512
        ks = 4
        return {
            "q": _w((lambda *s: torch.randn(*s, dtype=torch.bfloat16) * 0.05)(B, S, D)),
            "k": _w((lambda *s: torch.randn(*s, dtype=torch.bfloat16) * 0.8)(B, S, D)),
            "v": _w((lambda *s: torch.randn(*s, dtype=torch.bfloat16) * 0.8)(B, S, D)),
            "shift_k": _w((lambda *s: torch.randn(*s, dtype=torch.bfloat16) * 0.8)(D, ks) * 0.5),
            "ssm_k": _w((lambda *s: torch.randn(*s, dtype=torch.bfloat16) * 0.8)(D, S) * (1.0 / S ** 0.5)),
            "ssm_D": _w((lambda *s: torch.randn(*s, dtype=torch.bfloat16) * 0.8)(D)),
        }
    _A_in = _A_setup()

    def _B_setup():
        B, S, D, H = 1, 2048, 512, 32
        Dh = D // H
        return {
            "x": _w((lambda *s: torch.randn(*s, dtype=torch.bfloat16) * 0.05)(B, S, D)),
            "wqkv": _w((lambda *s: torch.randn(*s, dtype=torch.bfloat16) * 0.4)(D, 3 * D)),
            "w_phi": _w((lambda *s: torch.randn(*s, dtype=torch.bfloat16) * 0.4)(H, Dh, Dh) * (Dh ** -0.5)),
            "wo": _w((lambda *s: torch.randn(*s, dtype=torch.bfloat16) * 0.2)(D, D)),
        }
    _B_in = _B_setup()

    return {
        "A_q": _A_in["q"],
        "A_k": _A_in["k"],
        "A_v": _A_in["v"],
        "A_shift_k": _A_in["shift_k"],
        "A_ssm_k": _A_in["ssm_k"],
        "A_ssm_D": _A_in["ssm_D"],
        "B_x": _B_in["x"],
        "B_wqkv": _B_in["wqkv"],
        "B_w_phi": _B_in["w_phi"],
        "B_wo": _B_in["wo"],
    }

def reference(A_q, A_k, A_v, A_shift_k, A_ssm_k, A_ssm_D, B_x, B_wqkv, B_w_phi, B_wo):
    def _A_ref(q, k, v, shift_k, ssm_k, ssm_D):
        B, S, D = q.shape
        ks = shift_k.shape[-1]
        k_pad = _pad(k, ((0, 0), (ks - 1, 0), (0, 0)))
        k_shift = torch.zeros_like(k)
        for t in range(ks):
            k_shift = k_shift + k_pad[:, t:t + S, :] * shift_k[:, ks - 1 - t][None, None, :]
        kv = k_shift * v
        L = 2 * S
        kv_pad = _pad(kv, ((0, 0), (0, S), (0, 0)))
        kf_pad = _pad(ssm_k, ((0, 0), (0, S)))
        kv_f = (lambda x, axes=(-2,-1): torch.fft.rfft2(x.float(), dim=axes))(kv_pad[:, None, :, :], axes=(1, 2))
        kf_f = (lambda x, axes=(-2,-1): torch.fft.rfft2(x.float(), dim=axes))(kf_pad[:, None, :], axes=(1, 2))
        kf_f = (lambda x, *d: x.permute(*d))(kf_f, 1, 2, 0)
        y_f = kv_f * kf_f[None, :, :, :]
        y = (lambda x, s, axes=(-2,-1): torch.fft.irfft2(x, s=s, dim=axes).to(torch.bfloat16))(y_f, (1, L), axes=(1, 2))[:, 0, :S, :]
        y = y + kv * ssm_D[None, None, :]
        return q * y

    def _B_ref(x, wqkv, w_phi, wo):
        B, S, D = x.shape
        H = 32; Dh = D // H
        qkv = (lambda x, *d: x.permute(*d))((x @ wqkv).reshape(B, S, 3, H, Dh), 2, 0, 3, 1, 4)
        q, k, v = qkv[0], qkv[1], qkv[2]
        pq = torch.einsum("bhsd,hdf->bhsf", q, w_phi)
        pk = torch.einsum("bhsd,hdf->bhsf", k, w_phi)
        phi_q = torch.concatenate([_softmax(pq, axis=-1), _softmax(-pq, axis=-1)], axis=-1)
        phi_k = torch.concatenate([_softmax(pk, axis=-1), _softmax(-pk, axis=-1)], axis=-1)
        kv = torch.einsum("bhsf,bhsd->bhfd", phi_k, v)
        z  = torch.einsum("bhsf->bhf", phi_k)
        num = torch.einsum("bhqf,bhfd->bhqd", phi_q, kv)
        den = torch.einsum("bhqf,bhf->bhq", phi_q, z)[..., None] + 1e-6
        o = num / den
        return (lambda x, *d: x.permute(*d))(o, 0, 2, 1, 3).reshape(B, S, D) @ wo

    x = A_q
    x = x + _A_ref(_layernorm(x, axis=-1), A_k, A_v, A_shift_k, A_ssm_k, A_ssm_D)
    x = x + _B_ref(_layernorm(x, axis=-1), B_wqkv, B_w_phi, B_wo)
    x = x + _A_ref(_layernorm(x, axis=-1), A_k, A_v, A_shift_k, A_ssm_k, A_ssm_D)
    x = x + _B_ref(_layernorm(x, axis=-1), B_wqkv, B_w_phi, B_wo)
    return x
```

```cpp
#include <hip/hip_runtime.h>
#include <hip/hip_bf16.h>
#include <stdint.h>

typedef unsigned short u16;
__device__ __forceinline__ float b2f(u16 x) { return __uint_as_float(((unsigned)x) << 16); }
__device__ __forceinline__ u16 f2b(float f) { __bf16 b = (__bf16)f; return __builtin_bit_cast(u16, b); }
__device__ __forceinline__ float rb(float f) { return b2f(f2b(f)); }

constexpr int S = 2048, D = 512, H = 32, DH = 16;

__global__ void k_kv(const u16* k, const u16* v, const u16* sk, u16* kvT) {
    int i = blockIdx.x * 256 + threadIdx.x;
    int s = i / D, d = i % D;
    float acc = 0.f;
    for (int j = 3; j >= 0; --j) {
        float kk = (s - j >= 0) ? b2f(k[(s - j) * D + d]) : 0.f;
        acc = rb(acc + rb(kk * b2f(sk[d * 4 + j])));
    }
    kvT[d * S + s] = f2b(acc * b2f(v[i]));
}
__global__ void k_conv(const u16* kvT, const u16* h, const u16* Dv, u16* y) {
    int d = blockIdx.x;
    __shared__ float su[S], sh[S];
    for (int i = threadIdx.x; i < S; i += 256) { su[i] = b2f(kvT[d * S + i]); sh[i] = b2f(h[d * S + i]); }
    __syncthreads();
    for (int s = threadIdx.x; s < S; s += 256) {
        float acc = 0.f;
        for (int j = 0; j <= s; ++j) acc += su[j] * sh[s - j];
        y[s * D + d] = f2b(rb(acc) + rb(su[s] * b2f(Dv[d])));
    }
}
__device__ float blocksum(float v, float* red) {
    for (int o = 32; o > 0; o >>= 1) v += __shfl_xor(v, o);
    __syncthreads();
    if ((threadIdx.x & 63) == 0) red[threadIdx.x >> 6] = v;
    __syncthreads();
    float r = 0; for (int i = 0; i < 8; ++i) r += red[i];
    return r;
}
__global__ void k_ln2(const u16* x, const u16* y, u16* x1, u16* hout) {
    __shared__ float red[8];
    int s = blockIdx.x, d = threadIdx.x;
    float xv = b2f(x[s * D + d]);
    float m = blocksum(xv, red) / D;
    float var = blocksum((xv - m) * (xv - m), red) / D;
    float n = rb((xv - m) * rsqrtf(var + 1e-5f));
    float a = rb(xv + rb(n * b2f(y[s * D + d])));
    x1[s * D + d] = f2b(a);
    m = blocksum(a, red) / D;
    var = blocksum((a - m) * (a - m), red) / D;
    hout[s * D + d] = f2b((a - m) * rsqrtf(var + 1e-5f));
}
__global__ void k_gemm(const u16* A, const u16* B, const u16* res, u16* C, int N) {
    int s = blockIdx.x;
    __shared__ float sa[D];
    for (int i = threadIdx.x; i < D; i += 256) sa[i] = b2f(A[s * D + i]);
    __syncthreads();
    for (int n = threadIdx.x; n < N; n += 256) {
        float acc = 0;
        for (int k = 0; k < D; ++k) acc += sa[k] * b2f(B[k * N + n]);
        if (res) acc = rb(acc) + b2f(res[s * N + n]);
        C[s * N + n] = f2b(acc);
    }
}
__global__ void k_phi(const u16* qkv, const u16* wphi, u16* phi  ) {
    int i = blockIdx.x * 256 + threadIdx.x;
    int h = i % H, s = (i / H) % S, w = i / (S * H);
    float p[16];
    #pragma unroll
    for (int f = 0; f < 16; ++f) {
        float a = 0;
        for (int d = 0; d < 16; ++d) a += b2f(qkv[s * 3 * D + w * D + h * 16 + d]) * b2f(wphi[h * 256 + d * 16 + f]);
        p[f] = rb(a);
    }
    float mx = -1e30f, mn = 1e30f;
    #pragma unroll
    for (int f = 0; f < 16; ++f) { mx = fmaxf(mx, p[f]); mn = fminf(mn, p[f]); }
    float s1 = 0, s2 = 0;
    #pragma unroll
    for (int f = 0; f < 16; ++f) { s1 += __expf(p[f] - mx); s2 += __expf(mn - p[f]); }
    #pragma unroll
    for (int f = 0; f < 16; ++f) {
        phi[(size_t)i * 32 + f] = f2b(__expf(p[f] - mx) / s1);
        phi[(size_t)i * 32 + 16 + f] = f2b(__expf(mn - p[f]) / s2);
    }
}
__global__ void k_state(const u16* qkv, const u16* phi, float* st) {
    int h = blockIdx.x; int t = threadIdx.x;
    int f = t / 17, d = t % 17;
    const u16* pk = phi + (size_t)S * H * 32;
    float a = 0;
    for (int s = 0; s < S; ++s) {
        float vv = d < 16 ? b2f(qkv[s * 3 * D + 2 * D + h * 16 + d]) : 1.f;
        a += b2f(pk[(s * H + h) * 32 + f]) * vv;
    }
    st[h * 544 + t] = rb(a);
}
__global__ void k_o(const u16* phi, const float* st, u16* o) {
    int s = blockIdx.x, c = threadIdx.x; int h = c / 16, d = c % 16;
    float num = 0, den = 0;
    for (int f = 0; f < 32; ++f) {
        float p = b2f(phi[(s * H + h) * 32 + f]);
        num += p * st[h * 544 + f * 17 + d];
        den += p * st[h * 544 + f * 17 + 16];
    }
    o[s * D + c] = f2b(rb(num) / rb(rb(den) + 1e-6f));
}

extern "C" void kernel_launch(void* const* d_in, const int* in_sizes, int n_in,
                              void* d_out, int out_size, void* d_ws, size_t ws_size,
                              hipStream_t stream) {
    const u16* q = (const u16*)d_in[0]; const u16* k = (const u16*)d_in[1]; const u16* v = (const u16*)d_in[2];
    const u16* sk = (const u16*)d_in[3]; const u16* ssmk = (const u16*)d_in[4]; const u16* ssmD = (const u16*)d_in[5];
    const u16* wqkv = (const u16*)d_in[7]; const u16* wphi = (const u16*)d_in[8]; const u16* wo = (const u16*)d_in[9];
    char* ws = (char*)d_ws;
    const size_t MB = 1 << 20;
    u16* kvT = (u16*)(ws); u16* y = (u16*)(ws + 2 * MB); u16* x1 = (u16*)(ws + 4 * MB); u16* hh = (u16*)(ws + 6 * MB);
    u16* qkv = (u16*)(ws + 8 * MB); u16* phi = (u16*)(ws + 16 * MB); float* st = (float*)(ws + 26 * MB);
    u16* o = (u16*)(ws + 28 * MB); u16* x2 = (u16*)(ws + 30 * MB);
    k_kv<<<S * D / 256, 256, 0, stream>>>(k, v, sk, kvT);
    k_conv<<<D, 256, 0, stream>>>(kvT, ssmk, ssmD, y);
    const u16* xin = q;
    for (int l = 0; l < 2; ++l) {
        k_ln2<<<S, 512, 0, stream>>>(xin, y, x1, hh);
        k_gemm<<<S, 256, 0, stream>>>(hh, wqkv, nullptr, qkv, 3 * D);
        k_phi<<<2 * S * H / 256, 256, 0, stream>>>(qkv, wphi, phi);
        k_state<<<H, 544, 0, stream>>>(qkv, phi, st);
        k_o<<<S, 512, 0, stream>>>(phi, st, o);
        u16* dst = l == 0 ? x2 : (u16*)d_out;
        k_gemm<<<S, 256, 0, stream>>>(o, wo, x1, dst, D);
        xin = x2;
    }
}
```

```cpp
#include <hip/hip_runtime.h>
#include <stdint.h>

typedef unsigned u32;
typedef unsigned short u16;
typedef unsigned long long u64;
typedef u32 u32x4 __attribute__((ext_vector_type(4)));
typedef u32 u32x2 __attribute__((ext_vector_type(2)));
typedef float f32x2 __attribute__((ext_vector_type(2)));
typedef float f32x4 __attribute__((ext_vector_type(4)));
typedef float f32x16 __attribute__((ext_vector_type(16)));
typedef __bf16 bf16x2 __attribute__((ext_vector_type(2)));
typedef __bf16 bf16x8 __attribute__((ext_vector_type(8)));
typedef short s16x4 __attribute__((ext_vector_type(4)));
typedef __amdgpu_buffer_rsrc_t rsrc_t;
#define LAS __attribute__((address_space(3)))
#define DI __device__ __forceinline__

#define DLY_X1 12
#define DLY_ST 10
#define DLY_O 12
#define DLY_DS 12
#define DLY_X3 12

constexpr int S = 2048, D = 512;
constexpr u32 MB2 = 2u << 20;
constexpr u32 WS_WQ = 0, WS_WO = 0x180000, WS_KVP = MB2, WS_X0P = 2 * MB2, WS_YP = 3 * MB2, WS_X1P = 4 * MB2, WS_X3P = 5 * MB2,
              WS_OF = 6 * MB2, WS_SP = 8 * MB2, SP_UNIT = 2304, WS_DS = WS_SP + 2 * 16 * 16 * SP_UNIT, WS_TAILCLR = 5632,
              WS_CTRL = WS_SP + 0x180000, WS_E = WS_CTRL, WS_F1 = WS_CTRL + 4096, WS_F2 = WS_F1 + 32768, WS_ST0 = WS_F2 + 16384,
              WS_STAMP = 9 * MB2, WS_BYTES = 10 * MB2;
static_assert(WS_DS + 64 * 16 * 256 == WS_SP + 256 * WS_TAILCLR, "tail clear");
constexpr u32 L_W = 0, L_R2 = 3 * 33 * 1024;
constexpr u32 L_HC = L_R2, HC_COPY = 4160, HC_CH = 4 * HC_COPY, L_UR = L_HC + 2 * HC_CH, UR_CH = 79 * 96, L_YC = L_UR + 2 * UR_CH, YC_CH = 64 * 72;
constexpr u32 L_PK = 0  , L_PV = 72 * 128, L_PX = L_PV + 64 * 128;
constexpr u32 L_IMG = L_R2, IMG_RS = 224, L_O = L_R2, L_KVT = L_R2 + 32768, KVT_RS = 80, L_Z = L_KVT + 32 * KVT_RS, L_ZERO = L_Z + 128;
constexpr u32 LDS_BYTES = 163840;
static_assert(L_YC + 2 * YC_CH <= LDS_BYTES && L_ZERO + 64 <= LDS_BYTES, "lds");
constexpr int SC1 = 16;
constexpr u32 SENT = 0xFFFFFFFFu;
constexpr u32 SPIN_CAP = 1u << 15;

DI u32 pk(float a, float b) { return __builtin_bit_cast(u32, __builtin_convertvector(f32x2{a, b}, bf16x2)); }
DI float lo(u32 x) { return __builtin_bit_cast(float, x << 16); }
DI float hi(u32 x) { return __builtin_bit_cast(float, x & 0xFFFF0000u); }
DI float rb(float x) { return lo(pk(x, x)); }
DI float dot2(u32 a, u32 b, float c) { return __builtin_amdgcn_fdot2_f32_bf16(__builtin_bit_cast(bf16x2, a), __builtin_bit_cast(bf16x2, b), c, false); }
DI f32x16 mfma32(u32x4 a, u32x4 b, f32x16 c) { return __builtin_amdgcn_mfma_f32_32x32x16_bf16(__builtin_bit_cast(bf16x8, a), __builtin_bit_cast(bf16x8, b), c, 0, 0, 0); }
DI f32x4 mfma16(u32x4 a, u32x4 b, f32x4 c) { return __builtin_amdgcn_mfma_f32_16x16x32_bf16(__builtin_bit_cast(bf16x8, a), __builtin_bit_cast(bf16x8, b), c, 0, 0, 0); }
DI f32x4 mfma4(u32x2 a, u32x2 b, f32x4 c) { return __builtin_amdgcn_mfma_f32_4x4x4bf16_1k(__builtin_bit_cast(s16x4, a), __builtin_bit_cast(s16x4, b), c, 0, 0, 0); }
DI rsrc_t mkrs(const void* p, u32 bytes) { return __builtin_amdgcn_make_buffer_rsrc((void*)p, 0, bytes, 0x00020000); }
template <int AUX> DI u32x4 ld4(rsrc_t r, u32 v, u32 s) { return __builtin_amdgcn_raw_buffer_load_b128(r, v, s, AUX); }
template <int AUX> DI u32 ld1(rsrc_t r, u32 v, u32 s) { return __builtin_amdgcn_raw_buffer_load_b32(r, v, s, AUX); }
template <int AUX> DI void st4(u32x4 x, rsrc_t r, u32 v, u32 s) { __builtin_amdgcn_raw_buffer_store_b128(x, r, v + s, 0, AUX); }
template <int AUX> DI void st2(u32x2 x, rsrc_t r, u32 v, u32 s) { __builtin_amdgcn_raw_buffer_store_b64(x, r, v, s, AUX); }
template <int AUX> DI void st1(u32 x, rsrc_t r, u32 v, u32 s) { __builtin_amdgcn_raw_buffer_store_b32(x, r, v, s, AUX); }
DI void dma(rsrc_t r, u32 v, u32 s, u32 ldsaddr) {
    asm volatile("s_nop 4\n\ts_mov_b32 m0, %0\n\ts_nop 0\n\tbuffer_load_dwordx4 %1, %2, %3 offen lds" ::"s"(ldsaddr), "v"(v), "s"(r), "s"(s) : "memory");
}
DI void dma_sc1(rsrc_t r, u32 v, u32 s, u32 ldsaddr) {
    asm volatile("s_nop 4\n\ts_mov_b32 m0, %0\n\ts_nop 0\n\tbuffer_load_dwordx4 %1, %2, %3 offen sc1 lds" ::"s"(ldsaddr), "v"(v), "s"(r), "s"(s) : "memory");
}
template <typename T> DI T lds_ld(u32 a) { return *(LAS T*)(size_t)a; }
template <typename T> DI void lds_st(u32 a, T v) { *(LAS T*)(size_t)a = v; }
DI u32x2 lds_tr(u32 a) { return __builtin_bit_cast(u32x2, __builtin_amdgcn_ds_read_tr16_b64_v4i16((LAS s16x4*)(size_t)a)); }
DI void bar() { asm volatile("s_waitcnt lgkmcnt(0)\n\ts_barrier" ::: "memory"); }
DI void drain() { asm volatile("s_waitcnt vmcnt(0)" ::: "memory"); }
DI void fence() { asm volatile("" ::: "memory"); }
template <int N> DI void delay() { if (N > 0) __builtin_amdgcn_s_sleep(N); }
DI u32 nos(u32 x) { return x < 0xFFFFFFFEu ? x : 0xFFFFFFFEu; }
DI u32x4 nos4(u32x4 x) { return u32x4{nos(x.x), nos(x.y), nos(x.z), nos(x.w)}; }
DI u32 max4(u32 m, u32x4 v) { return max(max(m, max(v.x, v.y)), max(v.z, v.w)); }
DI float fasu(u32 x) { return __builtin_bit_cast(float, x); }
DI u32 uasf(float x) { return __builtin_bit_cast(u32, x); }
DI float halfsum(float x) { auto r = __builtin_amdgcn_permlane32_swap(uasf(x), uasf(x), false, false); return fasu(r[0]) + fasu(r[1]); }
DI float rowpairsum(float x) { auto r = __builtin_amdgcn_permlane16_swap(uasf(x), uasf(x), false, false); return fasu(r[0]) + fasu(r[1]); }
template <int CTRL> DI float dppadd(float x) { return x + fasu(__builtin_amdgcn_update_dpp(0u, uasf(x), CTRL, 0xf, 0xf, true)); }
DI float wavesum(float x) {
    x = dppadd<0xB1>(x); x = dppadd<0x4E>(x); x = dppadd<0x124>(x); x = dppadd<0x128>(x);
    return halfsum(rowpairsum(x));
}
DI u32x4 t1(u32x4 a) {
    auto r02 = __builtin_amdgcn_permlane16_swap(a.x, a.z, false, false);
    auto r13 = __builtin_amdgcn_permlane16_swap(a.y, a.w, false, false);
    u32 a0 = r02[0], a2 = r02[1], a1 = r13[0], a3 = r13[1];
    u32x4 o;
    o.x = __builtin_amdgcn_update_dpp(a0, a1, 0x128, 0xf, 0xc, false);
    o.y = __builtin_amdgcn_update_dpp(a1, a0, 0x128, 0xf, 0x3, false);
    o.z = __builtin_amdgcn_update_dpp(a2, a3, 0x128, 0xf, 0xc, false);
    o.w = __builtin_amdgcn_update_dpp(a3, a2, 0x128, 0xf, 0x3, false);
    return o;
}
DI void lnstat(float sum, float sq, float& m, float& s) {
    float mean = sum * (1.f / D);
    float var = fmaxf(sq * (1.f / D) - mean * mean, 0.f);
    m = rb(mean);
    s = rb(__builtin_amdgcn_sqrtf(rb(rb(var) + 1e-5f)));
}
constexpr u32 ONE_LO = 0x00003F80u, ONE_HI = 0x3F800000u, ONE2 = 0x3F803F80u;

DI void hedgehog(const f32x16& p, float rs, u32 (&o)[16]) {
    float mx = p[0], mn = p[0];
#pragma unroll
    for (int i = 1; i < 16; ++i) { mx = fmaxf(mx, p[i]); mn = fminf(mn, p[i]); }
    const float c = rs * 1.44269504088896f;
    const float a = -mx * c, b = mn * c;
    float e[16], sum = 0.f;
#pragma unroll
    for (int i = 0; i < 16; ++i) { e[i] = __builtin_amdgcn_exp2f(__builtin_fmaf(p[i], c, a)); sum += e[i]; }
    float r = __builtin_amdgcn_rcpf(sum);
#pragma unroll
    for (int i = 0; i < 8; ++i) o[i] = pk(e[2 * i] * r, e[2 * i + 1] * r);
    sum = 0.f;
#pragma unroll
    for (int i = 0; i < 16; ++i) { e[i] = __builtin_amdgcn_exp2f(__builtin_fmaf(p[i], -c, b)); sum += e[i]; }
    r = __builtin_amdgcn_rcpf(sum);
#pragma unroll
    for (int i = 0; i < 8; ++i) o[8 + i] = pk(e[2 * i] * r, e[2 * i + 1] * r);
}

#define STAMP(i)
#define COUNT(i, v)

__global__ void __launch_bounds__(512, 2) h3_hedgehog_kernel(const u16* gq, const u16* gk, const u16* gv, const u16* gsk, const u16* gh, const u16* gD,
                                                             const u16* gW, const u16* gphi, const u16* gwo, u16* gout, char* ws) {
    extern __shared__ __attribute__((aligned(16))) char smem[];
    const u32 tid = threadIdx.x, lane = tid & 63, wave = __builtin_amdgcn_readfirstlane(tid >> 6), b = blockIdx.x;
    const u32 xcd = b & 7, slot = b >> 3;
    const u32 L0 = (u32)(size_t)(LAS char*)smem;
    const rsrc_t rw = mkrs(ws, WS_BYTES);
    STAMP(0);
    const rsrc_t rq = mkrs(gq, S * D * 2), rk = mkrs(gk, S * D * 2), rv = mkrs(gv, S * D * 2);
    {
        const u32 r8 = lane >> 3, c = lane & 7;
        const u32 row = wave * 8 + r8;
        const u32 g0 = (slot * 64 + row) * 1024 + xcd * 128;
        dma(rk, g0 + ((c ^ (((row + 8) >> 2) & 7)) << 4), 0, L0 + L_PK + (wave + 1) * 1024);
        dma(rv, g0 + ((c ^ ((row >> 2) & 7)) << 4), 0, L0 + L_PV + wave * 1024);
        if (wave == 0) dma(rk, slot == 0 ? 0x80000000u : g0 - 8192 + ((c ^ ((r8 >> 2) & 7)) << 4), 0, L0 + L_PK);
        dma(rq, g0 + ((c ^ ((row >> 2) & 7)) << 4), 0, L0 + L_PX + wave * 1024);
    }
    const u32 pp = tid >> 4, tq = tid & 15;
    const u32x4 wsk = ld4<0>(mkrs(gsk, D * 4 * 2), (xcd * 32 + pp) * 16, 0);
    fence();
    const u32 tag = *(const u32*)(ws + WS_E) + 1u;
    const u32 pair = xcd * 32 + slot;
    const rsrc_t rh = mkrs(gh + (size_t)pair * 2 * S, 2 * S * 2);
    const u32 hch = tid >> 8, hq = tid & 255;
    const u32x4 h0 = ld4<0>(rh, hch * 4096 + hq * 16, 0);
    const u32x4 h1 = ld4<0>(rh, hq == 255 ? 0x80000000u : hch * 4096 + hq * 16 + 16, 0);
    const u32x4 xrow = ld4<0>(rq, (b * 8 + wave) * 1024 + lane * 16, 0);
    u32x4 wa0, wa1; u32 ph[8];
    const u32 kb = b & 15, cq = b >> 4, rho = lane & 31, hh = lane >> 5;
    if (wave >= 4) {
        const u32 v = wave - 4;
        if (cq < 12) {
            const u32 krow = (rho & 0x13) | ((rho & 4) << 1) | ((rho & 8) >> 1);
            const rsrc_t r = mkrs(gW, D * 3 * D * 2);
            const u32 o = (kb * 32 + krow) * 3072 + (cq * 4 + v) * 64 + hh * 16;
            wa0 = ld4<0>(r, o, 0); wa1 = ld4<0>(r, o + 32, 0);
        } else {
            const u32 krow = (rho & 3) | ((rho & 4) << 2) | ((rho & 16) >> 1) | ((rho & 8) >> 1);
            const rsrc_t r = mkrs(gwo, D * D * 2);
            const u32 o = (kb * 32 + krow) * 1024 + ((cq - 12) * 4 + v) * 64 + hh * 16;
            wa0 = ld4<0>(r, o, 0); wa1 = ld4<0>(r, o + 32, 0);
        }
        if (cq < 8) {
            const u32 head = (((cq & 3) * 4 + v) << 1) + ((rho >> 2) & 1), f = (rho & 3) + 4 * (rho >> 3);
#pragma unroll
            for (int e = 0; e < 8; ++e) ph[e] = gphi[head * 256 + (hh * 8 + e) * 16 + f];
        }
    }
    fence();
    {
        const u32x4 s4 = {SENT, SENT, SENT, SENT};
#pragma unroll
        for (int i = 0; i < 4; ++i) st4<SC1>(s4, rw, b * 8192 + tid * 16, WS_X1P + i * MB2);
        st4<SC1>(s4, rw, tid < WS_TAILCLR / 16 ? b * WS_TAILCLR + tid * 16 : 0x80000000u, WS_SP);
    }
    if (tid < 180) { const u32 c = tid >= 90; lds_st<u32x4>(L0 + L_UR + c * UR_CH + 64 * 96 + (tid - c * 90) * 16, u32x4{0, 0, 0, 0}); }
    asm volatile("" ::"v"(wsk) : "memory");
    STAMP(1);
    bar();
    {
        const u32 cw = (pp & 3) * 4, ch = pp >> 2;
        const u32 a1 = L0 + L_PK + (4 * tq + 4) * 128 + ((ch ^ ((tq + 1) & 7)) << 4) + cw;
        const u32 a2 = L0 + L_PK + (4 * tq + 8) * 128 + ((ch ^ ((tq + 2) & 7)) << 4) + cw;
        const u32 a3 = L0 + (4 * tq) * 128 + ((ch ^ (tq & 7)) << 4) + cw;
        u32 kk[7], vv[4]; u32x4 xx;
#pragma unroll
        for (int i = 0; i < 3; ++i) kk[i] = lds_ld<u32>(a1 + (i + 1) * 128);
#pragma unroll
        for (int i = 0; i < 4; ++i) { kk[3 + i] = lds_ld<u32>(a2 + i * 128); vv[i] = lds_ld<u32>(a3 + L_PV + i * 128); xx[i] = lds_ld<u32>(a3 + L_PX + i * 128); }
        const float wl[4] = {lo(wsk.x), hi(wsk.x), lo(wsk.y), hi(wsk.y)}, wh[4] = {lo(wsk.z), hi(wsk.z), lo(wsk.w), hi(wsk.w)};
        u32x4 kv;
#pragma unroll
        for (int i = 0; i < 4; ++i) {
            float sl = dot2(pk(lo(kk[i]) * wl[3], lo(kk[i + 1]) * wl[2]), ONE2, 0.f);
            float sh = dot2(pk(hi(kk[i]) * wh[3], hi(kk[i + 1]) * wh[2]), ONE2, 0.f);
            sl = dot2(pk(sl, lo(kk[i + 2]) * wl[1]), ONE2, 0.f);
            sh = dot2(pk(sh, hi(kk[i + 2]) * wh[1]), ONE2, 0.f);
            sl = dot2(pk(sl, lo(kk[i + 3]) * wl[0]), ONE2, 0.f);
            sh = dot2(pk(sh, hi(kk[i + 3]) * wh[0]), ONE2, 0.f);
            const u32 ks = pk(sl, sh);
            kv[i] = pk(lo(ks) * lo(vv[i]), hi(ks) * hi(vv[i]));
        }
        const u32 o = (xcd * 32 + pp) * 8192 + (slot * 64 + 4 * tq) * 4;
        st4<SC1>(kv, rw, o, WS_KVP);
        st4<SC1>(xx, rw, o, WS_X0P);
    }
    STAMP(2);
    {
        float sum = 0.f, sq = 0.f;
#pragma unroll
        for (int i = 0; i < 4; ++i) { sum = dot2(xrow[i], ONE2, sum); sq = dot2(xrow[i], xrow[i], sq); }
        sum = wavesum(sum); sq = wavesum(sq);
        float m, s; lnstat(sum, sq, m, s);
        st1<SC1>(pk(m, s), rw, lane == 0 ? (b * 8 + wave) * 4 : 0x80000000u, WS_ST0);
    }
    if (wave >= 4) {
        const u32 v = wave - 4;
        u32x4 bf;
        u32 sel;
        if (cq < 8) {
            sel = (rho >> 2) & 1;
            bf = u32x4{ph[0] | (ph[1] << 16), ph[2] | (ph[3] << 16), ph[4] | (ph[5] << 16), ph[6] | (ph[7] << 16)};
        } else {
            const u32 n = cq < 12 ? (((rho >> 2) & 1) * 16 + (rho & 3) + 4 * (rho >> 3)) : ((rho & 0x13) | ((rho & 4) << 1) | ((rho & 8) >> 1));
            sel = n >> 4;
            const u32 e = (n & 15) - hh * 8;
            const u32 one = (e & 1) ? ONE_HI : ONE_LO;
            bf = u32x4{(e >> 1) == 0 ? one : 0u, (e >> 1) == 1 ? one : 0u, (e >> 1) == 2 ? one : 0u, (e >> 1) == 3 ? one : 0u};
        }
        const u32x4 z4 = {0, 0, 0, 0};
        f32x16 acc = {};
        acc = mfma32(wa0, sel == 0 ? bf : z4, acc);
        acc = mfma32(wa1, sel == 1 ? bf : z4, acc);
        const u32 t = cq * 4 + v;
        const u32 o = (cq < 12 ? WS_WQ + (((t & 15) * 3 + (t >> 4)) * 32 + kb * 2) * 1024 : WS_WO + ((t - 48) * 32 + kb * 2) * 1024);
#pragma unroll
        for (int s = 0; s < 2; ++s)
            st4<SC1>(u32x4{pk(acc[8 * s], acc[8 * s + 1]), pk(acc[8 * s + 2], acc[8 * s + 3]), pk(acc[8 * s + 4], acc[8 * s + 5]), pk(acc[8 * s + 6], acc[8 * s + 7])},
                     rw, lane * 16 + s * 1024, o);
    }
    {
        auto put = [&](u32 ch, u32 oct, u32x4 a, u32x4 c) {
            const u32 d[8] = {a.x, a.y, a.z, a.w, c.x, c.y, c.z, c.w};
            u32 al[5];
#pragma unroll
            for (int i = 0; i < 5; ++i) al[i] = __builtin_amdgcn_alignbit(d[i + 1], d[i], 16);
            const u32 p = L0 + L_HC + ch * HC_CH + oct * 16;
            lds_st<u32x4>(p, u32x4{al[0], al[1], al[2], al[3]});
            lds_st<u32x4>(p + HC_COPY, u32x4{d[1], d[2], d[3], d[4]});
            lds_st<u32x4>(p + 2 * HC_COPY, u32x4{al[1], al[2], al[3], al[4]});
            lds_st<u32x4>(p + 3 * HC_COPY, u32x4{d[2], d[3], d[4], d[5]});
        };
        put(hch, hq + 4, h0, h1);
        if (hq < 4) { const u32x4 z4 = {0, 0, 0, 0}; put(hch, 3 - hq, z4, hq == 0 ? h0 : z4); }
    }
    drain();
    bar();
    STAMP(3);
    if (wave == 0) st1<SC1>(tag, rw, lane < 32 ? (xcd + 8 * lane) * 128 + slot * 4 : 0x80000000u, WS_F1);
    if (wave == 1) st1<SC1>(tag, rw, lane < 4 ? lane * 4096 + (b >> 2) * 64 + (b & 3) * 4 : 0x80000000u, WS_F2);

    const u32 grp = xcd * 2 + (slot & 1), rank = slot >> 1;
    const u32 g16 = lane >> 4, l16 = lane & 15;
    const u32 selv = ((l16 >> 2) & 1) == (g16 & 1) ? ONE2 : 0u;
    const u32x4 sumsel = {selv, selv, selv, selv};

    if (wave < 4) {
        bar();
        STAMP(4);
        {
            const u32 ch = wave >> 1, it = wave & 1;
            u32 aA = L0 + L_HC + ch * HC_CH + (l16 & 3) * HC_COPY + 2 * (16 * it + 8 * g16 + (l16 & 12));
            u32 aA2 = aA + 8;
            asm volatile("" : "+v"(aA2));
            u32 aB = L0 + L_UR + ch * UR_CH + (15 - l16) * 96 + 16 * g16;
            f32x4 acc[4] = {};
            u32x4 A[2][4], U[2][4];
            auto rd = [&](int buf, int n) {
#pragma unroll
                for (int i = 0; i < 4; ++i) {
                    const u32x2 x = lds_ld<u32x2>(aA + i * 1024 + n * 64), y = lds_ld<u32x2>(aA2 + i * 1024 + n * 64);
                    A[buf][i] = u32x4{x.x, x.y, y.x, y.y};
                    U[buf][i] = lds_ld<u32x4>(aB + (3 - i) * 1536 + n * 96);
                }
            };
            auto mm = [&](int buf) {
#pragma unroll
                for (int be = 0; be < 4; ++be)
#pragma unroll
                    for (int c = be; c < 4; ++c) acc[c] = mfma16(A[buf][be], U[buf][c - be], acc[c]);
            };
            rd(0, 0);
#pragma unroll 1
            for (int n = 0; n < 7; ++n) {
                rd(1, 1); mm(0); rd(0, 2); mm(1);
                aA += 128; aA2 += 128; aB += 192;
            }
            rd(1, 1); mm(0); mm(1);
#pragma unroll
            for (int c = 0; c < 4; ++c)
                lds_st<u32x2>(L0 + L_YC + ch * YC_CH + (16 * c + l16) * 72 + (16 * it + 4 * g16) * 2, u32x2{pk(acc[c][0], acc[c][1]), pk(acc[c][2], acc[c][3])});
        }
        STAMP(5);
        bar();
        bar();
        const u32 T = grp * 4 + wave;
        const u32 xoff = (lane >> 3) * 8192 + (T * 32 + 4 * (lane & 7)) * 4;
        const u32 aW = L0 + L_W + lane * 16;
#pragma unroll 1
        for (int layer = 0; layer < 2; ++layer) {
            STAMP(6 + 10 * layer);
            if (layer == 0) delay<DLY_X1>(); else delay<DLY_X3>();
            const u32 xb = layer == 0 ? WS_X1P : WS_X3P;
            u32x4 X[32];
            f32x16 aK, aV; f32x4 aS; float sq;
            u32 tries = 0;
            while (true) {
                fence();
#pragma unroll
                for (int i = 0; i < 32; ++i) X[i] = ld4<SC1>(rw, xoff, xb + i * 65536);
                __builtin_amdgcn_sched_barrier(0);
                aK = f32x16{}; aV = f32x16{}; aS = f32x4{};
                f32x4 a4 = {}, a5 = {};
#pragma unroll
                for (int i = 0; i < 32; ++i) {
                    const u32x4 wk = lds_ld<u32x4>(aW + (33 + i) * 1024), wv = lds_ld<u32x4>(aW + (66 + i) * 1024);
                    X[i] = t1(X[i]);
                    aK = mfma32(wk, X[i], aK);
                    a4 = mfma4(u32x2{X[i].x, X[i].y}, u32x2{X[i].x, X[i].y}, a4);
                    aS = mfma16(sumsel, X[i], aS);
                    aV = mfma32(wv, X[i], aV);
                    a5 = mfma4(u32x2{X[i].z, X[i].w}, u32x2{X[i].z, X[i].w}, a5);
                }
                a4 += a5;
                const u32 q = lane & 3;
                sq = halfsum(q == 0 ? a4[0] : q == 1 ? a4[1] : q == 2 ? a4[2] : a4[3]);
                if (!__any(sq != sq)) break;
                u32 m = 0;
#pragma unroll
                for (int i = 0; i < 32; ++i) m = max4(m, X[i]);
                if (!__any(m == SENT) || ++tries > 4096) break;
            }
            COUNT(28 + layer, tries);
            STAMP(7 + 10 * layer);
            bar();
            float mean, sd; lnstat(aS[0], sq, mean, sd);
            const u32 nm = pk(-mean, -mean);
            const u32x4 xm = {nm, nm & 0xFFFFu, 0, 0};
            aK = mfma32(lds_ld<u32x4>(aW + 65 * 1024), xm, aK);
            aV = mfma32(lds_ld<u32x4>(aW + 98 * 1024), xm, aV);
            const float rs = __builtin_amdgcn_rcpf(sd);
            const u32 aI = L0 + L_IMG + (wave * 32 + rho) * IMG_RS;
#pragma unroll
            for (int s = 0; s < 2; ++s)
                lds_st<u32x4>(aI + 128 + hh * 32 + s * 16, u32x4{pk(aV[8 * s] * rs, aV[8 * s + 1] * rs), pk(aV[8 * s + 2] * rs, aV[8 * s + 3] * rs),
                                                                 pk(aV[8 * s + 4] * rs, aV[8 * s + 5] * rs), pk(aV[8 * s + 6] * rs, aV[8 * s + 7] * rs)});
            u32 phi[16];
            hedgehog(aK, rs, phi);
#pragma unroll
            for (int s = 0; s < 4; ++s) lds_st<u32x4>(aI + hh * 64 + s * 16, u32x4{phi[4 * s], phi[4 * s + 1], phi[4 * s + 2], phi[4 * s + 3]});
            STAMP(8 + 10 * layer);
            bar();
            f32x16 aQ = {};
#pragma unroll
            for (int i = 0; i < 32; ++i) aQ = mfma32(lds_ld<u32x4>(aW + i * 1024), X[i], aQ);
            aQ = mfma32(lds_ld<u32x4>(aW + 32 * 1024), xm, aQ);
            hedgehog(aQ, rs, phi);
            STAMP(9 + 10 * layer);
            bar();
            STAMP(10 + 10 * layer);
            {
                const u32 aN = ((rho >> 2) & 1) == hh ? L0 + L_KVT + (hh * 16 + (rho & 3) + 4 * (rho >> 3)) * KVT_RS : L0 + L_ZERO;
                const u32 aZ = (l16 >> 2) == g16 ? L0 + L_Z + (g16 >> 1) * 64 : L0 + L_ZERO;
                f32x16 num = {}; f32x4 den = {};
#pragma unroll
                for (int s = 0; s < 4; ++s) {
                    const u32x4 p = {phi[4 * s], phi[4 * s + 1], phi[4 * s + 2], phi[4 * s + 3]};
                    den = mfma16(lds_ld<u32x4>(aZ + s * 16), p, den);
                    num = mfma32(lds_ld<u32x4>(aN + s * 16), p, num);
                }
                const float r = __builtin_amdgcn_rcpf(den[0] + 1e-6f);
#pragma unroll
                for (int s = 0; s < 2; ++s)
                    st4<SC1>(nos4(u32x4{pk(num[8 * s] * r, num[8 * s + 1] * r), pk(num[8 * s + 2] * r, num[8 * s + 3] * r),
                                        pk(num[8 * s + 4] * r, num[8 * s + 5] * r), pk(num[8 * s + 6] * r, num[8 * s + 7] * r)}),
                             rw, lane * 16 + s * 1024, WS_OF + ((layer * 64 + T) * 32 + 2 * rank) * 1024);
            }
            STAMP(11 + 10 * layer);
            bar();
            delay<DLY_O>();
            {
                const u32 ob = WS_OF + ((layer * 64 + grp * 4 + (rank >> 2)) * 32 + wave * 4) * 1024;
                u32x4 o[4]; u32 sp = 0;
                while (true) {
                    fence();
                    u32 m = 0;
#pragma unroll
                    for (int i = 0; i < 4; ++i) o[i] = ld4<SC1>(rw, lane * 16 + i * 1024, ob);
#pragma unroll
                    for (int i = 0; i < 4; ++i) m = max4(m, o[i]);
                    if (!__any(m == SENT) || ++sp > SPIN_CAP) break;
                }
                COUNT(30 + layer, sp);
#pragma unroll
                for (int i = 0; i < 4; ++i) lds_st<u32x4>(L0 + L_O + (wave * 4 + i) * 1024 + lane * 16, o[i]);
            }
            STAMP(12 + 10 * layer);
            bar();
            bar();
        }
    } else {
        const u32 v = wave - 4, dt = tid - 256;
        u32x4 kv[2], x0[2];
        {
            u32 sp = 0;
            while (true) {
                fence();
                const u32 f = ld1<SC1>(rw, b * 128 + (lane & 31) * 4, WS_F1);
                if (__all(f == tag) || ++sp > SPIN_CAP) break;
            }
            fence();
            COUNT(26, sp);
            STAMP(4);
#pragma unroll
            for (int i = 0; i < 2; ++i) kv[i] = ld4<SC1>(rw, pair * 8192 + dt * 16 + i * 4096, WS_KVP);
#pragma unroll
            for (int i = 0; i < 2; ++i) x0[i] = ld4<SC1>(rw, pair * 8192 + dt * 16 + i * 4096, WS_X0P);
#pragma unroll
            for (int i = 0; i < 2; ++i) {
                const u32 i0 = 2044 - 4 * (dt + 256 * i);
                const u32 a = L0 + L_UR + (i0 >> 5) * 96 + (i0 & 31) * 2;
                lds_st<u32x2>(a, u32x2{__builtin_amdgcn_perm(kv[i].z, kv[i].w, 0x05040100u), __builtin_amdgcn_perm(kv[i].x, kv[i].y, 0x05040100u)});
                lds_st<u32x2>(a + UR_CH, u32x2{__builtin_amdgcn_perm(kv[i].z, kv[i].w, 0x07060302u), __builtin_amdgcn_perm(kv[i].x, kv[i].y, 0x07060302u)});
            }
        }
        bar();
        STAMP(5);
        {
            u32 sp = 0;
            while (true) {
                fence();
                const u32x4 f = ld4<SC1>(rw, (b & 3) * 4096 + lane * 64, WS_F2);
                if (__all(f.x == tag && f.y == tag && f.z == tag && f.w == tag) || ++sp > SPIN_CAP) break;
                __builtin_amdgcn_s_sleep(2);
            }
            fence();
            COUNT(27, sp);
        }
        STAMP(13);
        u32x4 st0[2];
#pragma unroll
        for (int i = 0; i < 2; ++i) st0[i] = ld4<SC1>(rw, dt * 16 + i * 4096, WS_ST0);
        const u32 dD = *(const u32*)(gD + 2 * pair);
        fence();
#pragma unroll 1
        for (u32 i = 0; i < 24; ++i) {
            u32 f = v + 4 * i + 32; f = f >= 96 ? f - 96 : f;
            dma_sc1(rw, lane * 16, WS_WQ + (rank * 96 + f) * 1024, L0 + L_W + (f + (f >> 5)) * 1024);
        }
        const u32 ct = rank & 3, T = grp * 4 + (rank >> 2), tile = ct * 4 + v;
        u32x4 WO[32];
#pragma unroll
        for (int i = 0; i < 32; ++i) WO[i] = ld4<SC1>(rw, lane * 16, WS_WO + (tile * 32 + i) * 1024);
        __builtin_amdgcn_sched_barrier(0);
        {
            float tD[16], xf[16]; u32 nm[16];
#pragma unroll
            for (int i = 0; i < 8; ++i) {
                const u32 k = kv[i >> 2][i & 3], x = x0[i >> 2][i & 3], st = st0[i >> 2][i & 3];
                const u32 t = pk(lo(k) * lo(dD), hi(k) * hi(dD));
                tD[2 * i] = lo(t); tD[2 * i + 1] = hi(t);
                xf[2 * i] = lo(x); xf[2 * i + 1] = hi(x);
                const u32 d = pk(lo(x) - lo(st), hi(x) - lo(st));
                const float rs = __builtin_amdgcn_rcpf(hi(st));
                const u32 n = pk(lo(d) * rs, hi(d) * rs);
                nm[2 * i] = n & 0xFFFFu; nm[2 * i + 1] = n & 0xFFFF0000u;
            }
            u32 selLo = ONE_LO, selHi = ONE_HI;
            asm volatile("" : "+s"(selLo), "+s"(selHi));
            STAMP(14);
            bar();
            STAMP(15);
#pragma unroll
            for (int i = 0; i < 2; ++i) {
                const u32 t0 = 4 * (dt + 256 * i);
                const u32 a = L0 + L_YC + (t0 >> 5) * 72 + (t0 & 31) * 2;
                const u32x2 ya = lds_ld<u32x2>(a), yb = lds_ld<u32x2>(a + YC_CH);
                u32x4 y, x1;
#pragma unroll
                for (int e = 0; e < 4; ++e) {
                    const int j = 4 * i + e;
                    const u32 sel = (e & 1) ? selHi : selLo;
                    y[e] = pk(dot2(ya[e >> 1], sel, tD[2 * j]), dot2(yb[e >> 1], sel, tD[2 * j + 1]));
                    const u32 p = pk(dot2(y[e], nm[2 * j], 0.f), dot2(y[e], nm[2 * j + 1], 0.f));
                    x1[e] = nos(pk(dot2(p, selLo, xf[2 * j]), dot2(p, selHi, xf[2 * j + 1])));
                }
                st4<SC1>(x1, rw, pair * 8192 + dt * 16 + i * 4096, WS_X1P);
                st4<SC1>(y, rw, pair * 8192 + dt * 16 + i * 4096, WS_YP);
            }
        }
        asm volatile("s_waitcnt vmcnt(4)" ::: "memory");
        STAMP(25);
        bar();
        if (v < 3) {
            f32x4 cs = {};
#pragma unroll 4
            for (int i = 0; i < 32; ++i) cs = mfma16(sumsel, lds_ld<u32x4>(L0 + L_W + (v * 33 + i) * 1024 + lane * 16), cs);
            const float c0 = rb(cs[0]), c1 = rb(cs[0] - c0), c2 = cs[0] - c0 - c1;
            lds_st<u32x4>(L0 + L_W + (v * 33 + 32) * 1024 + lane * 16, hh ? u32x4{0, 0, 0, 0} : u32x4{pk(c0, c1), pk(c2, 0.f), 0, 0});
        } else if (lane < 4) lds_st<u32x4>(L0 + L_ZERO + lane * 16, u32x4{0, 0, 0, 0});
        const u32 poff = ((tile * 16 + (lane >> 3)) * 2048 + T * 32 + 4 * (lane & 7)) * 4;
        u32x4 xr[2];
#pragma unroll 1
        for (int layer = 0; layer < 2; ++layer) {
            bar();
            drain();
            bar();
            STAMP(6 + 10 * layer);
            const u32 head = v >> 1, fh = v & 1;
            const u32 spb = WS_SP + (layer * 16 + rank) * 16 * SP_UNIT;
            {
                const u32 a = L0 + L_IMG + (4 * g16 + (l16 >> 2)) * IMG_RS + (l16 & 3) * 8;
                f32x4 kvp = {}, zp = {};
                const u32x4 ones = {ONE2, ONE2, ONE2, ONE2};
#pragma unroll
                for (int ks = 0; ks < 4; ++ks) {
                    const u32 r0 = a + ks * 32 * IMG_RS, r1 = r0 + 16 * IMG_RS;
                    const u32x2 p0 = lds_tr(r0 + head * 64 + fh * 32), p1 = lds_tr(r1 + head * 64 + fh * 32);
                    const u32x2 v0 = lds_tr(r0 + 128 + head * 32), v1 = lds_tr(r1 + 128 + head * 32);
                    const u32x4 pa = {p0.x, p0.y, p1.x, p1.y};
                    kvp = mfma16(pa, u32x4{v0.x, v0.y, v1.x, v1.y}, kvp);
                    zp = mfma16(pa, ones, zp);
                }
                st2<SC1>(u32x2{nos(pk(kvp[0], kvp[1])), nos(pk(kvp[2], kvp[3]))}, rw, v * 512 + l16 * 32 + g16 * 8, spb + grp * SP_UNIT);
                st4<SC1>(nos4(__builtin_bit_cast(u32x4, zp)), rw, l16 == 0 ? 2048 + v * 64 + g16 * 16 : 0x80000000u, spb + grp * SP_UNIT);
            }
            STAMP(7 + 10 * layer);
            if (layer == 0) {
#pragma unroll
                for (int s = 0; s < 2; ++s) xr[s] = ld4<SC1>(rw, poff, WS_X1P + s * 65536);
#pragma unroll
                for (int s = 0; s < 2; ++s) xr[s] = t1(xr[s]);
            } else delay<DLY_ST>();
            {
                u32x4 pt[8]; u32 zt[4]; u32 sp = 0;
                while (true) {
                    fence();
#pragma unroll
                    for (int i = 0; i < 8; ++i) pt[i] = ld4<SC1>(rw, (g16 >> 1) * SP_UNIT + v * 512 + l16 * 32 + (g16 & 1) * 16, spb + 2 * i * SP_UNIT);
#pragma unroll
                    for (int i = 0; i < 4; ++i) zt[i] = ld1<SC1>(rw, g16 * SP_UNIT + 2048 + v * 64 + l16 * 4, spb + 4 * i * SP_UNIT);
                    u32 m = max(max(zt[0], zt[1]), max(zt[2], zt[3]));
#pragma unroll
                    for (int i = 0; i < 8; ++i) m = max4(m, pt[i]);
                    if (!__any(m == SENT) || ++sp > SPIN_CAP) break;
                }
                COUNT(28 + layer, sp);
                const u32 e = l16 & 7, one = (l16 >> 3) == (g16 & 1) ? ((e & 1) ? ONE_HI : ONE_LO) : 0u;
                const u32x4 idm = {(e >> 1) == 0 ? one : 0u, (e >> 1) == 1 ? one : 0u, (e >> 1) == 2 ? one : 0u, (e >> 1) == 3 ? one : 0u};
                f32x4 t = {};
#pragma unroll
                for (int i = 0; i < 8; ++i) t = mfma16(idm, pt[i], t);
                const float z = halfsum(rowpairsum((fasu(zt[0]) + fasu(zt[1])) + (fasu(zt[2]) + fasu(zt[3]))));
                lds_st<u32x2>(L0 + L_KVT + (head * 16 + l16) * KVT_RS + fh * 32 + g16 * 8, u32x2{pk(t[0], t[1]), pk(t[2], t[3])});
                if (lane < 16) lds_st<u16>(L0 + L_Z + head * 64 + fh * 32 + lane * 2, (u16)pk(z, z));
            }
            STAMP(8 + 10 * layer);
            bar();
            bar();
            delay<DLY_O>();
            {
                const u32 ob = WS_OF + ((layer * 64 + T) * 32 + wave * 4) * 1024;
                u32x4 o[4]; u32 sp = 0;
                while (true) {
                    fence();
                    u32 m = 0;
#pragma unroll
                    for (int i = 0; i < 4; ++i) o[i] = ld4<SC1>(rw, lane * 16 + i * 1024, ob);
#pragma unroll
                    for (int i = 0; i < 4; ++i) m = max4(m, o[i]);
                    if (!__any(m == SENT) || ++sp > SPIN_CAP) break;
                }
                COUNT(30 + layer, sp);
#pragma unroll
                for (int i = 0; i < 4; ++i) lds_st<u32x4>(L0 + L_O + (wave * 4 + i) * 1024 + lane * 16, o[i]);
            }
            bar();
            STAMP(9 + 10 * layer);
            u32x4 yt[2];
            if (layer == 0) {
#pragma unroll
                for (int s = 0; s < 2; ++s) yt[s] = ld4<SC1>(rw, poff, WS_YP + s * 65536);
            }
            f32x16 acc = {};
#pragma unroll
            for (int i = 0; i < 32; ++i) acc = mfma32(WO[i], lds_ld<u32x4>(L0 + L_O + i * 1024 + lane * 16), acc);
            u32 x2[8];
#pragma unroll
            for (int i = 0; i < 8; ++i) { const u32 x = xr[i >> 2][i & 3]; x2[i] = pk(lo(x) + acc[2 * i], hi(x) + acc[2 * i + 1]); }
            STAMP(10 + 10 * layer);
            if (layer == 1) {
                const rsrc_t ro = mkrs(gout, S * D * 2);
                const u32 o = ((T * 32 + 4 * (rho & 7) + (rho >> 3)) * D + tile * 32 + hh * 8) * 2;
                st4<0>(u32x4{x2[0], x2[1], x2[2], x2[3]}, ro, o, 0);
                st4<0>(u32x4{x2[4], x2[5], x2[6], x2[7]}, ro, o + 32, 0);
            } else {
                float sum = 0.f, sq = 0.f;
#pragma unroll
                for (int i = 0; i < 8; ++i) { sum = dot2(x2[i], ONE2, sum); sq = dot2(x2[i], x2[i], sq); }
                auto r = __builtin_amdgcn_permlane32_swap(uasf(sum), uasf(sq), false, false);
                st1<SC1>(nos(uasf(fasu(r[0]) + fasu(r[1]))), rw, lane * 4, WS_DS + (T * 16 + tile) * 256);
#pragma unroll
                for (int s = 0; s < 2; ++s) yt[s] = t1(yt[s]);
                delay<DLY_DS>();
                u32 ds[16], sp = 0;
                while (true) {
                    fence();
                    u32 m = 0;
#pragma unroll
                    for (int i = 0; i < 16; ++i) ds[i] = ld1<SC1>(rw, lane * 4 + i * 256, WS_DS + T * 4096);
#pragma unroll
                    for (int i = 0; i < 16; ++i) m = max(m, ds[i]);
                    if (!__any(m == SENT) || ++sp > SPIN_CAP) break;
                }
                COUNT(24, sp);
                STAMP(11);
                float a = 0.f;
#pragma unroll
                for (int i = 0; i < 16; ++i) a += fasu(ds[i]);
                auto r2 = __builtin_amdgcn_permlane32_swap(uasf(a), uasf(a), false, false);
                float mean, sd; lnstat(fasu(r2[0]), fasu(r2[1]), mean, sd);
                const float rs = __builtin_amdgcn_rcpf(sd);
#pragma unroll
                for (int i = 0; i < 8; ++i) {
                    const u32 d = pk(lo(x2[i]) - mean, hi(x2[i]) - mean);
                    const u32 n = pk(lo(d) * rs, hi(d) * rs);
                    const u32 y = yt[i >> 2][i & 3];
                    const u32 p = pk(lo(n) * lo(y), hi(n) * hi(y));
                    xr[i >> 2][i & 3] = pk(lo(x2[i]) + lo(p), hi(x2[i]) + hi(p));
                }
#pragma unroll
                for (int s = 0; s < 2; ++s) st4<SC1>(nos4(t1(xr[s])), rw, poff, WS_X3P + s * 65536);
            }
            STAMP(12 + 10 * layer);
            bar();
        }
        if (b == 0 && tid == 256) *(u32*)(ws + WS_E) = tag;
    }
}

extern "C" void kernel_launch(void* const* d_in, const int* in_sizes, int n_in, void* d_out, int out_size, void* d_ws, size_t ws_size, hipStream_t stream) {
    (void)hipFuncSetAttribute((const void*)h3_hedgehog_kernel, hipFuncAttributeMaxDynamicSharedMemorySize, LDS_BYTES);
    h3_hedgehog_kernel<<<256, 512, LDS_BYTES, stream>>>((const u16*)d_in[0], (const u16*)d_in[1], (const u16*)d_in[2], (const u16*)d_in[3], (const u16*)d_in[4],
                                                        (const u16*)d_in[5], (const u16*)d_in[7], (const u16*)d_in[8], (const u16*)d_in[9], (u16*)d_out, (char*)d_ws);
}
```

```cpp
#include <hip/hip_runtime.h>
#include <stdint.h>

typedef unsigned u32;
typedef unsigned short u16;
typedef unsigned long long u64;
typedef u32 u32x4 __attribute__((ext_vector_type(4)));
typedef u32 u32x2 __attribute__((ext_vector_type(2)));
typedef float f32x2 __attribute__((ext_vector_type(2)));
typedef float f32x4 __attribute__((ext_vector_type(4)));
typedef float f32x16 __attribute__((ext_vector_type(16)));
typedef __bf16 bf16x2 __attribute__((ext_vector_type(2)));
typedef __bf16 bf16x8 __attribute__((ext_vector_type(8)));
typedef short s16x4 __attribute__((ext_vector_type(4)));
typedef __amdgpu_buffer_rsrc_t rsrc_t;
#define LAS __attribute__((address_space(3)))
#define DI __device__ __forceinline__

#define DLY_F1 14
#define DLY_S0 0
#define DLY_SC 10
#define XWIN 12
#define MAP48 0
#define H1SAME 1
#define WO_EARLY 12
#define Y_WAIT 45
#define H_LATE 0
#define DLY_ST 10
#define DLY_O 12
#define DLY_DS 8
#define DLY_X 12
#define CY 1
#define CX 2
#define CO 4
#define CZ 2
#define QW 2
#define BS_AT 20

constexpr int S = 2048, D = 512;
constexpr u32 MB2 = 2u << 20;
constexpr u32 WS_WQ = 0, WS_WO = 0x180000, WS_KVP = MB2, WS_YP = 3 * MB2, WS_XF = 4 * MB2,
              WS_OF = 6 * MB2, WS_SP = 8 * MB2, SP_UNIT = 2304, WS_DS = WS_SP + 2 * 16 * 16 * SP_UNIT, DS_BUF = 64 * 16 * 256,
              WS_STAMP = 9 * MB2, WS_CTRL = WS_STAMP + 0x100000, WS_E = WS_CTRL, WS_F1 = WS_CTRL + 4096, WS_FW = WS_F1 + 256 * 512, WS_F3 = WS_FW + 256 * 512,
              WS_ST0 = WS_F3 + 4 * 4096, WS_CS = WS_ST0 + 16384, WS_BYTES = 10 * MB2;
static_assert(WS_DS + 3 * DS_BUF <= WS_STAMP, "ws");
constexpr u32 OOB = 0x80000000u;
constexpr u32 L_W = 0, L_R2 = 3 * 33 * 1024;
constexpr u32 L_HC = L_R2, HC_COPY = 4160, HC_CH = 4 * HC_COPY, L_UR = L_HC + 2 * HC_CH, UR_CH = 79 * 96, L_YC = L_UR + 2 * UR_CH, YC_CH = 64 * 72;
constexpr u32 L_PK = 0  , L_PV = 72 * 128, L_PX = L_PV + 64 * 128;
constexpr u32 L_IMG = L_R2, IMG_RS = 224, L_O = L_R2, L_KVT = L_R2 + 32768, KVT_RS = 80, L_Z = L_KVT + 32 * KVT_RS, L_ZERO = L_Z + 128, L_LN = L_ZERO + 64;
constexpr u32 LDS_BYTES = 163840, L_CNT = LDS_BYTES - 16;
static_assert(L_YC + 2 * YC_CH <= LDS_BYTES && L_ZERO + 64 <= LDS_BYTES, "lds");
constexpr int SC1 = 16;
constexpr u32 SENT = 0xFFFFFFFFu;
constexpr u32 SPIN_CAP = 1u << 15;

DI u32 pk(float a, float b) { return __builtin_bit_cast(u32, __builtin_convertvector(f32x2{a, b}, bf16x2)); }
DI float lo(u32 x) { return __builtin_bit_cast(float, x << 16); }
DI float hi(u32 x) { return __builtin_bit_cast(float, x & 0xFFFF0000u); }
DI float rb(float x) { return lo(pk(x, x)); }
DI float dot2(u32 a, u32 b, float c) { return __builtin_amdgcn_fdot2_f32_bf16(__builtin_bit_cast(bf16x2, a), __builtin_bit_cast(bf16x2, b), c, false); }
DI f32x2 pfma(f32x2 a, f32x2 b, f32x2 c) { return __builtin_elementwise_fma(a, b, c); }
DI f32x2 rb2(f32x2 x) { return pfma(x, f32x2{-65536.f, -65536.f}, x * 65537.f); }
DI f32x2 up2(u32 x) { return f32x2{lo(x), hi(x)}; }
DI u32 pk2(f32x2 x) { return pk(x.x, x.y); }
DI f32x16 mfma32(u32x4 a, u32x4 b, f32x16 c) { return __builtin_amdgcn_mfma_f32_32x32x16_bf16(__builtin_bit_cast(bf16x8, a), __builtin_bit_cast(bf16x8, b), c, 0, 0, 0); }
DI f32x4 mfma16(u32x4 a, u32x4 b, f32x4 c) { return __builtin_amdgcn_mfma_f32_16x16x32_bf16(__builtin_bit_cast(bf16x8, a), __builtin_bit_cast(bf16x8, b), c, 0, 0, 0); }
DI rsrc_t mkrs(const void* p, u32 bytes) { return __builtin_amdgcn_make_buffer_rsrc((void*)p, 0, bytes, 0x00020000); }
template <int AUX> DI u32x4 ld4(rsrc_t r, u32 v, u32 s) { return __builtin_amdgcn_raw_buffer_load_b128(r, v, s, AUX); }
template <int AUX> DI u32 ld1(rsrc_t r, u32 v, u32 s) { return __builtin_amdgcn_raw_buffer_load_b32(r, v, s, AUX); }
template <int AUX> DI void st4(u32x4 x, rsrc_t r, u32 v, u32 s) { __builtin_amdgcn_raw_buffer_store_b128(x, r, v + s, 0, AUX); }
template <int AUX> DI void st2(u32x2 x, rsrc_t r, u32 v, u32 s) { __builtin_amdgcn_raw_buffer_store_b64(x, r, v, s, AUX); }
template <int AUX> DI void st1(u32 x, rsrc_t r, u32 v, u32 s) { __builtin_amdgcn_raw_buffer_store_b32(x, r, v, s, AUX); }
DI void dma(rsrc_t r, u32 v, u32 s, u32 ldsaddr) {
    asm volatile("s_nop 4\n\ts_mov_b32 m0, %0\n\ts_nop 0\n\tbuffer_load_dwordx4 %1, %2, %3 offen lds" ::"s"(ldsaddr), "v"(v), "s"(r), "s"(s) : "memory");
}
template <typename T> DI T lds_ld(u32 a) { return *(LAS T*)(size_t)a; }
template <typename T> DI void lds_st(u32 a, T v) { *(LAS T*)(size_t)a = v; }
DI u32x2 lds_tr(u32 a) { return __builtin_bit_cast(u32x2, __builtin_amdgcn_ds_read_tr16_b64_v4i16((LAS s16x4*)(size_t)a)); }
DI void bar() { asm volatile("s_waitcnt lgkmcnt(0)\n\ts_barrier" ::: "memory"); }
DI void drain() { asm volatile("s_waitcnt vmcnt(0)" ::: "memory"); }
DI void fence() { asm volatile("" ::: "memory"); }
DI bool spent(u32& sp) { u32 one; asm volatile("s_mov_b32 %0, 1" : "=s"(one)); sp += one; return sp > SPIN_CAP; }
template <int N> DI void delay() { if (N > 0) __builtin_amdgcn_s_sleep(N); }
DI u32 nos(u32 x) { return x < 0xFFFFFFFEu ? x : 0xFFFFFFFEu; }
DI u32x4 nos4(u32x4 x) { return u32x4{nos(x.x), nos(x.y), nos(x.z), nos(x.w)}; }
DI u32 max4(u32 m, u32x4 v) { return max(max(max(max(m, v.x), v.y), v.z), v.w); }
DI float fasu(u32 x) { return __builtin_bit_cast(float, x); }
DI u32 uasf(float x) { return __builtin_bit_cast(u32, x); }
DI float halfsum(float x) { auto r = __builtin_amdgcn_permlane32_swap(uasf(x), uasf(x), false, false); return fasu(r[0]) + fasu(r[1]); }
DI float rowpairsum(float x) { auto r = __builtin_amdgcn_permlane16_swap(uasf(x), uasf(x), false, false); return fasu(r[0]) + fasu(r[1]); }
template <int CTRL> DI float dppadd(float x) { return x + fasu(__builtin_amdgcn_update_dpp(0u, uasf(x), CTRL, 0xf, 0xf, true)); }
DI u32x4 t1(u32x4 a) {
    auto r02 = __builtin_amdgcn_permlane16_swap(a.x, a.z, false, false);
    auto r13 = __builtin_amdgcn_permlane16_swap(a.y, a.w, false, false);
    u32 a0 = r02[0], a2 = r02[1], a1 = r13[0], a3 = r13[1];
    u32x4 o;
    o.x = __builtin_amdgcn_update_dpp(a0, a1, 0x128, 0xf, 0xc, false);
    o.y = __builtin_amdgcn_update_dpp(a1, a0, 0x128, 0xf, 0x3, false);
    o.z = __builtin_amdgcn_update_dpp(a2, a3, 0x128, 0xf, 0xc, false);
    o.w = __builtin_amdgcn_update_dpp(a3, a2, 0x128, 0xf, 0x3, false);
    return o;
}
#define SB() __builtin_amdgcn_sched_barrier(0)
DI void lnstat(float sum, float sq, float& m, float& s) {
    float mean = sum * (1.f / D);
    float var = fmaxf(sq * (1.f / D) - mean * mean, 0.f);
    m = rb(mean);
    s = rb(__builtin_amdgcn_sqrtf(rb(rb(var) + 1e-5f)));
}
constexpr u32 ONE_LO = 0x00003F80u, ONE_HI = 0x3F800000u, ONE2 = 0x3F803F80u;

DI void softmax16(const f32x16& p, float c, float a, u32* o) {
    f32x2 e[8];
    const f32x2 c2 = {c, c}, a2 = {a, a};
#pragma unroll
    for (int i = 0; i < 8; ++i) {
        const f32x2 t = __builtin_elementwise_fma(f32x2{p[2 * i], p[2 * i + 1]}, c2, a2);
        e[i] = f32x2{__builtin_amdgcn_exp2f(t.x), __builtin_amdgcn_exp2f(t.y)};
    }
    const f32x2 s = ((e[0] + e[1]) + (e[2] + e[3])) + ((e[4] + e[5]) + (e[6] + e[7]));
    const float r = __builtin_amdgcn_rcpf(s.x + s.y);
#pragma unroll
    for (int i = 0; i < 8; ++i) { const f32x2 t = e[i] * r; o[i] = pk(t.x, t.y); }
}
DI void hedgehog(const f32x16& p, float rs, u32 (&o)[16]) {
    const float mx = fmaxf(fmaxf(fmaxf(fmaxf(p[0], p[1]), p[2]), fmaxf(fmaxf(p[3], p[4]), p[5])), fmaxf(fmaxf(fmaxf(fmaxf(p[6], p[7]), p[8]), fmaxf(fmaxf(p[9], p[10]), p[11])), fmaxf(fmaxf(fmaxf(p[12], p[13]), p[14]), p[15])));
    const float mn = fminf(fminf(fminf(fminf(p[0], p[1]), p[2]), fminf(fminf(p[3], p[4]), p[5])), fminf(fminf(fminf(fminf(p[6], p[7]), p[8]), fminf(fminf(p[9], p[10]), p[11])), fminf(fminf(fminf(p[12], p[13]), p[14]), p[15])));
    const float c = rs * 1.44269504088896f;
    softmax16(p, c, -mx * c, &o[0]);
    softmax16(p, -c, mn * c, &o[8]);
}

#define STAMP(i)
#define COUNT(i, v)
#define DUMP()

template <int N, int BACKOFF> DI u32 wait_flags(rsrc_t r, u32 v, u32 s, u32 tag) {
    u32 sp = 0;
#pragma nounroll
    while (true) {
        fence();
        bool ok;
        if (N == 1) ok = ld1<SC1>(r, v, s) == tag;
        else { const u32x4 f = ld4<SC1>(r, v, s); ok = f.x == tag && f.y == tag && f.z == tag && f.w == tag; }
        if (__all(ok) || spent(sp)) break;
        delay<BACKOFF>();
    }
    fence();
    return sp;
}
template <int N, int BACKOFF, int STRIDE> DI u32 gather4(u32x4 (&o)[N], rsrc_t r, u32 v, u32 s) {
    u32 sp = 0;
#pragma nounroll
    while (true) {
        asm volatile("" : "+v"(v) :: "memory");
        u32 m = 0;
#pragma unroll
        for (int i = 0; i < N; ++i) o[i] = STRIDE < 4096 ? ld4<SC1>(r, v + i * STRIDE, s) : ld4<SC1>(r, v, s + i * STRIDE);
#pragma unroll
        for (int i = 0; i < N; ++i) m = max4(m, o[i]);
        if (!__any(m == SENT) || spent(sp)) break;
        delay<BACKOFF>();
    }
    return sp;
}
DI void stats_publish(const u32x4 (&x)[2], rsrc_t r, u32 lane, u32 off) {
    float s0 = 0.f, s1 = 0.f, q0 = 0.f, q1 = 0.f;
#pragma unroll
    for (int i = 0; i < 4; ++i) { s0 = dot2(x[0][i], ONE2, s0); q0 = dot2(x[0][i], x[0][i], q0); s1 = dot2(x[1][i], ONE2, s1); q1 = dot2(x[1][i], x[1][i], q1); }
    auto p = __builtin_amdgcn_permlane32_swap(uasf(s0 + s1), uasf(q0 + q1), false, false);
    st1<SC1>(nos(uasf(fasu(p[0]) + fasu(p[1]))), r, (lane & 15) * 16 + (lane >> 4) * 4, off);
}
DI u32 stats_gather(rsrc_t r, u32 lane, u32 off, float& mean, float& rs) {
    u32x4 ds[4];
    const u32 sp = gather4<4, 2, 1024>(ds, r, lane * 16, off);
    const f32x4 a = (__builtin_bit_cast(f32x4, ds[0]) + __builtin_bit_cast(f32x4, ds[1])) + (__builtin_bit_cast(f32x4, ds[2]) + __builtin_bit_cast(f32x4, ds[3]));
    auto p0 = __builtin_amdgcn_permlane32_swap(uasf(a[0]), uasf(a[2]), false, false);
    auto p1 = __builtin_amdgcn_permlane32_swap(uasf(a[1]), uasf(a[3]), false, false);
    auto p2 = __builtin_amdgcn_permlane16_swap(uasf(fasu(p0[0]) + fasu(p0[1])), uasf(fasu(p1[0]) + fasu(p1[1])), false, false);
    const float t = fasu(p2[0]) + fasu(p2[1]);
    auto p = __builtin_amdgcn_permlane32_swap(uasf(t), uasf(t), false, false);
    float sd; lnstat(fasu(p[0]), fasu(p[1]), mean, sd);
    rs = __builtin_amdgcn_rcpf(sd) * 1.0000003f;
    return sp;
}

__global__ void __launch_bounds__(512, 2) h3_hedgehog_kernel(const u16* gq, const u16* gk, const u16* gv, const u16* gsk, const u16* gh, const u16* gD,
                                                             const u16* gW, const u16* gphi, const u16* gwo, u16* gout, char* ws) {
    extern __shared__ __attribute__((aligned(16))) char smem[];
    const u32 tid = threadIdx.x, lane = tid & 63, wave = __builtin_amdgcn_readfirstlane(tid >> 6), b = blockIdx.x;
    const u32 xcd = b & 7, slot = b >> 3;
    const u32 L0 = (u32)(size_t)(LAS char*)smem;
    asm volatile("" ::"s"(gq), "s"(gk), "s"(gv), "s"(gsk), "s"(gh), "s"(gD), "s"(gW), "s"(gphi), "s"(gwo), "s"(gout), "s"(ws));
    u64 pc0; u32 codelen;
    asm volatile("s_getpc_b64 %0\n.Lh3_here:\n\ts_mov_b32 %1, .Lh3_end0-.Lh3_here\n\ts_max_i32 %1, %1, .Lh3_end1-.Lh3_here\n\ts_max_i32 %1, %1, 0" : "=s"(pc0), "=s"(codelen) :: "scc");
    const rsrc_t rw = mkrs(ws, WS_BYTES);
    STAMP(0);
    const u32 tt = H1SAME ? slot : xcd * 4 + (slot >> 3), cg = H1SAME ? xcd : slot & 7;
    const u32 r8 = lane >> 3, c8 = lane & 7;
    {
        const rsrc_t rk = mkrs(gk, S * D * 2), rv = mkrs(gv, S * D * 2);
        const u32 row = wave * 8 + r8;
        const u32 g0 = (tt * 64 + row) * 1024 + cg * 128;
        dma(rk, g0 + ((c8 ^ (((row + 8) >> 2) & 7)) << 4), 0, L0 + L_PK + (wave + 1) * 1024);
        dma(rv, g0 + ((c8 ^ ((row >> 2) & 7)) << 4), 0, L0 + L_PV + wave * 1024);
        if (wave == 0) dma(rk, tt == 0 ? OOB : g0 - 8192 + ((c8 ^ ((r8 >> 2) & 7)) << 4), 0, L0 + L_PK);
    }
    const u32 pp = tid >> 4, tq = tid & 15;
    const u32x4 wsk = ld4<0>(mkrs(gsk, D * 4 * 2), (cg * 32 + pp) * 16, 0);
    const u32 tagv = ld1<0>(rw, 0, WS_E);
    fence();
    const rsrc_t rq = mkrs(gq, S * D * 2);
    const u32 pair = xcd * 32 + slot;
    const u32 grp = MAP48 ? (xcd >> 1) * 4 + (slot & 3) : xcd * 2 + (slot & 1);
    const u32 rank = MAP48 ? (xcd & 1) * 8 + (slot >> 2) : slot >> 1;
    const u32 rho = lane & 31, hh = lane >> 5, g16 = lane >> 4, l16 = lane & 15;
    const u32 w4 = wave & 3, Td = grp * 4 + (rank >> 2), tile = (rank & 3) * 4 + w4;
    const u32x4 sent4 = {SENT, SENT, SENT, SENT};
    const u32 qoff = pair * 8192 + w4 * 2048 + lane * 16;
    const u32 doff = lane < 16 ? (Td * 16 + tile) * 256 + lane * 16 : OOB;
    const u32 ooff = ((grp * 4 + w4) * 32 + 2 * rank) * 1024 + lane * 16;
    const u32 xoff = (Td * 32 + 2 * tile) * 1024 + lane * 16;
    auto clears = [&](int at) __attribute__((always_inline)) {
#pragma unroll
        for (int i = 0; i < 2; ++i) {
            if (CY == at) st4<SC1>(sent4, rw, qoff + i * 1024, WS_YP);
            if (CX == at) st4<SC1>(sent4, rw, xoff + i * 1024, WS_XF);
            if (CO == at) st4<SC1>(sent4, rw, ooff + i * 1024, WS_OF);
        }
        if (CZ == at) {
            const u32 soff = lane < 36 ? (rank * 16 + grp) * SP_UNIT + (lane < 32 ? w4 * 512 + lane * 16 : 2048 + w4 * 64 + (lane - 32) * 16) : OOB;
            st4<SC1>(sent4, rw, soff, WS_SP);
            st4<SC1>(sent4, rw, soff, WS_SP + 256 * SP_UNIT);
            st4<SC1>(sent4, rw, doff, WS_DS);
        }
        fence();
    };
    u32 tag, ofl;
    auto phase0 = [&]() __attribute__((always_inline)) {
        const u32 cw = (pp & 3) * 4, pch = pp >> 2;
        u32 a1 = L0 + L_PK + (4 * tq + 4) * 128 + ((pch ^ ((tq + 1) & 7)) << 4) + cw;
        u32 a2 = L0 + L_PK + (4 * tq + 8) * 128 + ((pch ^ ((tq + 2) & 7)) << 4) + cw;
        u32 a3 = L0 + L_PV + (4 * tq) * 128 + ((pch ^ (tq & 7)) << 4) + cw;
        u32 okv = WS_KVP + (cg * 32 + pp) * 8192 + (tt * 64 + 4 * tq) * 4;
        ofl = lane < 4 ? WS_F1 + ((cg * 32 + wave * 4 + lane) * 4 + (tt >> 3)) * 128 + (tt & 7) * 4 : OOB;
        f32x2 w1[4], w2[4];
#pragma unroll
        for (int j = 0; j < 4; ++j) {
            const f32x2 w = (j & 1) ? f32x2{hi(wsk[j >> 1]), hi(wsk[2 + (j >> 1)])} : f32x2{lo(wsk[j >> 1]), lo(wsk[2 + (j >> 1)])};
            w1[j] = w * 65537.f; w2[j] = w * -65536.f;
            asm volatile("" : "+v"(w1[j]), "+v"(w2[j]));
        }
        asm volatile("" : "+v"(a1), "+v"(a2), "+v"(a3), "+v"(okv), "+v"(ofl));
        STAMP(1);
        bar();
        if (wave >= 4) clears(1);
        u32 kk[7], vv[4];
#pragma unroll
        for (int i = 0; i < 3; ++i) kk[i] = lds_ld<u32>(a1 + (i + 1) * 128);
#pragma unroll
        for (int i = 0; i < 4; ++i) kk[3 + i] = lds_ld<u32>(a2 + i * 128);
#pragma unroll
        for (int i = 0; i < 4; ++i) vv[i] = lds_ld<u32>(a3 + i * 128);
        f32x2 kf[7], sm[4];
#pragma unroll
        for (int i = 0; i < 7; ++i) kf[i] = up2(kk[i]);
#pragma unroll
        for (int i = 0; i < 4; ++i) sm[i] = pfma(kf[i], w2[3], kf[i] * w1[3]);
#pragma unroll
        for (int j = 2; j >= 0; --j)
#pragma unroll
            for (int i = 0; i < 4; ++i) sm[i] = rb2(sm[i] + pfma(kf[i + 3 - j], w2[j], kf[i + 3 - j] * w1[j]));
        u32x4 kv;
#pragma unroll
        for (int i = 0; i < 4; ++i) kv[i] = pk2(sm[i] * up2(vv[i]));
        st4<SC1>(kv, rw, okv, 0);
        tag = __builtin_amdgcn_readfirstlane(tagv) + 1u;
        STAMP(2);
    };

    if (wave < 4) {
        u32x4 xrow[2], hA[2], wa0, wa1; u32x2 hB[2]; u32 ph[8];
        const rsrc_t rh = mkrs(gh + (size_t)pair * 2 * S, 2 * S * 2);
        const u32 srow = grp * 128 + rank * 8 + wave * 2 + hh;
        {
            const bool isW = wave < 3;
            const u32 krow = isW ? (rho & 0x13) | ((rho & 4) << 1) | ((rho & 8) >> 1) : (rho & 3) | ((rho & 4) << 2) | ((rho & 24) >> 1);
            const rsrc_t r = mkrs(isW ? gW : gwo, isW ? D * 3 * D * 2 : D * D * 2);
            const u32 o = (grp * 32 + krow) * (isW ? 3072 : 1024) + (isW ? wave * 1024 : 0) + rank * 64 + hh * 16;
            wa0 = ld4<0>(r, o, 0); wa1 = ld4<0>(r, o + 32, 0);
            const rsrc_t rp = mkrs(gphi, wave < 2 ? 32 * 256 * 2 : 0);
            const u32 head = rank * 2 + ((rho >> 2) & 1), f = (rho & 3) + 4 * (rho >> 3);
#pragma unroll
            for (int e = 0; e < 8; ++e) ph[e] = __builtin_amdgcn_raw_buffer_load_b16(rp, (head * 256 + hh * 128 + f) * 2 + e * 32, 0, 0);
#pragma unroll
            for (int i = 0; i < 2; ++i) xrow[i] = ld4<0>(rq, srow * 1024 + i * 512 + rho * 16, 0);
        }
        auto hload = [&]() __attribute__((always_inline)) {
#pragma unroll
            for (int i = 0; i < 2; ++i) {
                hA[i] = ld4<0>(rh, i * 4096 + tid * 16, 0);
                hB[i] = __builtin_amdgcn_raw_buffer_load_b64(rh, tid == 255 ? OOB : i * 4096 + tid * 16 + 16, 0, 0);
            }
        };
        if (H_LATE == 0) hload();
        fence();
        phase0();
        SB();
        {
            u32x4 bf; u32 sel;
            if (wave < 2) {
                sel = (rho >> 2) & 1;
                bf = u32x4{ph[0] | (ph[1] << 16), ph[2] | (ph[3] << 16), ph[4] | (ph[5] << 16), ph[6] | (ph[7] << 16)};
            } else {
                const u32 n = wave < 3 ? (((rho >> 2) & 1) * 16 + (rho & 3) + 4 * (rho >> 3)) : ((rho & 0x13) | ((rho & 4) << 1) | ((rho & 8) >> 1));
                sel = n >> 4;
                const u32 e = (n & 15) - hh * 8;
                const u32 one = (e & 1) ? ONE_HI : ONE_LO;
                bf = u32x4{(e >> 1) == 0 ? one : 0u, (e >> 1) == 1 ? one : 0u, (e >> 1) == 2 ? one : 0u, (e >> 1) == 3 ? one : 0u};
            }
            const u32x4 z4 = {0, 0, 0, 0};
            f32x16 acc = {};
            acc = mfma32(wa0, sel == 0 ? bf : z4, acc);
            acc = mfma32(wa1, sel == 1 ? bf : z4, acc);
            const u32 o = (wave < 3 ? WS_WQ + ((rank * 3 + wave) * 32 + grp * 2) * 1024 : WS_WO + (rank * 32 + grp * 2) * 1024);
            float cs = 0.f;
#pragma unroll
            for (int s = 0; s < 2; ++s) {
                const u32x4 w = {pk(acc[8 * s], acc[8 * s + 1]), pk(acc[8 * s + 2], acc[8 * s + 3]), pk(acc[8 * s + 4], acc[8 * s + 5]), pk(acc[8 * s + 6], acc[8 * s + 7])};
                st4<SC1>(w, rw, lane * 16 + s * 1024, o);
#pragma unroll
                for (int i = 0; i < 4; ++i) cs = dot2(w[i], ONE2, cs);
            }
            st1<SC1>(uasf(halfsum(cs)), rw, lane < 32 ? WS_CS + ((rank * 4 + wave) * 16 + grp) * 128 + lane * 4 : OOB, 0);
        }
        {
            float sum = 0.f, sq = 0.f;
#pragma unroll
            for (int j = 0; j < 8; ++j) { sum = dot2(xrow[j >> 2][j & 3], ONE2, sum); sq = dot2(xrow[j >> 2][j & 3], xrow[j >> 2][j & 3], sq); }
            sum = rowpairsum(dppadd<0x128>(dppadd<0x124>(dppadd<0x4E>(dppadd<0xB1>(sum)))));
            sq = rowpairsum(dppadd<0x128>(dppadd<0x124>(dppadd<0x4E>(dppadd<0xB1>(sq)))));
            float m, s; lnstat(sum, sq, m, s);
            st2<SC1>(u32x2{pk(m, s), tag}, rw, rho == 0 ? srow * 8 : OOB, WS_ST0);
        }
        STAMP(14);
        asm volatile("s_waitcnt vmcnt(4)" ::: "memory");
        STAMP(32);
        st1<SC1>(tag, rw, ofl, 0);
        asm volatile("s_waitcnt vmcnt(2)" ::: "memory");
        st1<SC1>(tag, rw, wave < 3 && lane < 16 ? WS_FW + ((rank * 16 + lane) * 4 + (grp >> 2)) * 128 + ((grp & 3) * 3 + wave) * 4 : OOB, 0);
        STAMP(3);
        if (H_LATE == 2) hload();
        {
            auto put = [&](u32 ch, u32 oct, u32x4 a, u32x2 c) {
                const u32 d[6] = {a.x, a.y, a.z, a.w, c.x, c.y};
                u32 al[5];
#pragma unroll
                for (int i = 0; i < 5; ++i) al[i] = __builtin_amdgcn_alignbit(d[i + 1], d[i], 16);
                const u32 p = L0 + L_HC + ch * HC_CH + oct * 16;
                lds_st<u32x4>(p, u32x4{al[0], al[1], al[2], al[3]});
                lds_st<u32x4>(p + HC_COPY, u32x4{d[1], d[2], d[3], d[4]});
                lds_st<u32x4>(p + 2 * HC_COPY, u32x4{al[1], al[2], al[3], al[4]});
                lds_st<u32x4>(p + 3 * HC_COPY, u32x4{d[2], d[3], d[4], d[5]});
            };
#pragma unroll 1
            for (int i = 0; i < 2; ++i) {
                const u32x4 a = i ? hA[1] : hA[0];
                put(i, tid + 4, a, i ? hB[1] : hB[0]);
                if (tid < 4) put(i, 3 - tid, u32x4{0, 0, 0, 0}, tid == 0 ? u32x2{a.x, a.y} : u32x2{0, 0});
            }
        }
        STAMP(13);
        bar();
        STAMP(4);
        const u32 tur = (u32)__builtin_amdgcn_s_memrealtime();
        {
            const u32 ch = wave >> 1, it = wave & 1;
            u32 aA = L0 + L_HC + ch * HC_CH + (l16 & 3) * HC_COPY + 2 * (16 * it + 8 * g16 + (l16 & 12));
            u32 aB = L0 + L_UR + ch * UR_CH + (15 - l16) * 96 + 16 * g16;
            f32x4 acc[4] = {};
            asm volatile(
                ".macro H3MM d, a, u\n\tv_mfma_f32_16x16x32_bf16 \\d, v[\\a:\\a+3], v[\\u:\\u+3], \\d\n.endm\n"
                ".macro H3RA r, i, n, o\n\tds_read_b64 v[\\r+4*\\i+2*\\o:\\r+4*\\i+2*\\o+1], %4 offset:1024*\\i+64*\\n+8*\\o\n.endm\n"
                ".macro H3RU r, i, n\n\tds_read_b128 v[\\r+16+4*\\i:\\r+16+4*\\i+3], %5 offset:1536*(3-\\i)+96*\\n\n.endm\n"
                ".macro H3STEP x, y, n\n"
                "s_waitcnt lgkmcnt(6)\n"
                "H3MM %0, \\x, \\x+16\n H3RA \\y, 0, \\n, 0\n H3RA \\y, 0, \\n, 1\n"
                "H3MM %1, \\x, \\x+20\n H3RU \\y, 0, \\n\n"
                "H3MM %2, \\x, \\x+24\n H3RU \\y, 1, \\n\n"
                "H3MM %3, \\x, \\x+28\n H3RU \\y, 2, \\n\n"
                "s_waitcnt lgkmcnt(9)\n"
                "H3MM %1, \\x+4, \\x+16\n H3RU \\y, 3, \\n\n"
                "H3MM %2, \\x+4, \\x+20\n H3RA \\y, 1, \\n, 0\n H3RA \\y, 1, \\n, 1\n"
                "H3MM %3, \\x+4, \\x+24\n H3RA \\y, 2, \\n, 0\n"
                "s_waitcnt lgkmcnt(9)\n"
                "H3MM %2, \\x+8, \\x+16\n H3RA \\y, 2, \\n, 1\n"
                "H3MM %3, \\x+8, \\x+20\n H3RA \\y, 3, \\n, 0\n"
                "H3MM %3, \\x+12, \\x+16\n H3RA \\y, 3, \\n, 1\n"
                ".endm\n"
                "H3RA 64, 0, 0, 0\n H3RA 64, 0, 0, 1\n H3RU 64, 0, 0\n H3RU 64, 1, 0\n H3RU 64, 2, 0\n H3RU 64, 3, 0\n"
                "H3RA 64, 1, 0, 0\n H3RA 64, 1, 0, 1\n H3RA 64, 2, 0, 0\n H3RA 64, 2, 0, 1\n H3RA 64, 3, 0, 0\n H3RA 64, 3, 0, 1\n"
                "s_mov_b32 s60, 4\n"
                ".Lh3_conv:\n"
                "H3STEP 64, 96, 1\n"
                "H3STEP 96, 64, 2\n"
                "H3STEP 64, 96, 3\n"
                "s_sub_u32 s60, s60, 1\n"
                "H3STEP 96, 64, 4\n"
                "v_add_u32 %4, 0x100, %4\n v_add_u32 %5, 0x180, %5\n"
                "s_cmp_lg_u32 s60, 0\n"
                "s_cbranch_scc1 .Lh3_conv\n"
                "s_waitcnt lgkmcnt(0)\n s_nop 7\n"
                ".purgem H3MM\n.purgem H3RA\n.purgem H3RU\n.purgem H3STEP\n"
                : "+v"(acc[0]), "+v"(acc[1]), "+v"(acc[2]), "+v"(acc[3]), "+v"(aA), "+v"(aB)
                :
                : "memory", "scc", "s60", "v64", "v65", "v66", "v67", "v68", "v69", "v70", "v71", "v72", "v73", "v74", "v75", "v76", "v77", "v78", "v79",
                  "v80", "v81", "v82", "v83", "v84", "v85", "v86", "v87", "v88", "v89", "v90", "v91", "v92", "v93", "v94", "v95",
                  "v96", "v97", "v98", "v99", "v100", "v101", "v102", "v103", "v104", "v105", "v106", "v107", "v108", "v109", "v110", "v111",
                  "v112", "v113", "v114", "v115", "v116", "v117", "v118", "v119", "v120", "v121", "v122", "v123", "v124", "v125", "v126", "v127");
#pragma unroll
            for (int c = 0; c < 4; ++c)
                lds_st<u32x2>(L0 + L_YC + ch * YC_CH + (16 * c + l16) * 72 + (16 * it + 4 * g16) * 2, u32x2{pk(acc[c][0], acc[c][1]), pk(acc[c][2], acc[c][3])});
        }
        STAMP(5);
        u32x4 f3[2];
        u32 f3o = wave == 0 ? WS_F3 + (b & 3) * 4096 + lane * 64 : OOB;
        f3[0] = ld4<SC1>(rw, f3o, 0); f3[1] = ld4<SC1>(rw, f3o + 16, 0);
        bar();
        if (wave == 0) {
            const u32 tyc = (u32)__builtin_amdgcn_s_memrealtime();
            u32 sp = 0;
            while (__any(((f3[0].y ^ tag) | (f3[0].w ^ tag) | (f3[1].y ^ tag) | (f3[1].w ^ tag)) != 0) && !spent(sp)) {
                delay<4>();
                asm volatile("" : "+v"(f3o) :: "memory");
                f3[0] = ld4<SC1>(rw, f3o, 0); f3[1] = ld4<SC1>(rw, f3o + 16, 0);
            }
            fence();
            COUNT(27, sp);
            STAMP(23);
            int lag = max(max((int)(f3[0].x - tur), (int)(f3[0].z - tur)), max((int)(f3[1].x - tur), (int)(f3[1].z - tur)));
            lag = max(lag, __builtin_amdgcn_update_dpp(0, lag, 0xB1, 0xf, 0xf, true));
            lag = max(lag, __builtin_amdgcn_update_dpp(0, lag, 0x4E, 0xf, 0xf, true));
            lag = max(lag, __builtin_amdgcn_update_dpp(0, lag, 0x124, 0xf, 0xf, true));
            lag = max(lag, __builtin_amdgcn_update_dpp(0, lag, 0x128, 0xf, 0xf, true));
            lag = max(max(__builtin_amdgcn_readlane(lag, 0), __builtin_amdgcn_readlane(lag, 16)), max(__builtin_amdgcn_readlane(lag, 32), __builtin_amdgcn_readlane(lag, 48)));
            lag = min(lag, 1000) + Y_WAIT;
            COUNT(33, lag);
            while ((int)((u32)__builtin_amdgcn_s_memrealtime() - tyc) < lag) __builtin_amdgcn_s_sleep(1);
        }
        STAMP(25);
        bar();
        delay<2>();
        if (QW == 2) {
            const u32 so = WS_WQ + (rank * 96 + wave * 8) * 1024, la = L0 + L_W + wave * 8192;
            asm volatile("s_nop 4\n\ts_mov_b32 m0, %0\n\ts_nop 0\n\t"
                         "buffer_load_dwordx4 %2, %3, %4 offen sc1 lds\n\tbuffer_load_dwordx4 %2, %3, %4 offen offset:1024 sc1 lds\n\t"
                         "buffer_load_dwordx4 %2, %3, %4 offen offset:2048 sc1 lds\n\tbuffer_load_dwordx4 %2, %3, %4 offen offset:3072 sc1 lds\n\t"
                         "s_mov_b32 m0, %1\n\ts_nop 0\n\t"
                         "buffer_load_dwordx4 %2, %3, %5 offen sc1 lds\n\tbuffer_load_dwordx4 %2, %3, %5 offen offset:1024 sc1 lds\n\t"
                         "buffer_load_dwordx4 %2, %3, %5 offen offset:2048 sc1 lds\n\tbuffer_load_dwordx4 %2, %3, %5 offen offset:3072 sc1 lds"
                         ::"s"(la), "s"(la + 4096), "v"(lane * 16), "s"(rw), "s"(so), "s"(so + 4096) : "memory");
        }
        clears(4);
        drain();
        STAMP(34);
        const u32 T = grp * 4 + wave;
        const u32 aW = L0 + L_W + lane * 16;
        u32 aW1 = aW + 33 * 1024, aW2 = aW + 66 * 1024;
        asm volatile("" : "+v"(aW1), "+v"(aW2));
#pragma unroll 1
        for (int layer = 0; layer < 2; ++layer) {
            bar();
            STAMP(6 + 10 * layer);
            delay<DLY_X>();
            const u32 xb = WS_XF + layer * MB2 + T * 32768;
            u32 xo = lane * 16;
            u32x4 X[32];
            f32x16 aK, aV;
            u32 tries = 0;
            while (true) {
                asm volatile("" : "+v"(xo) :: "memory");
#pragma unroll
                for (int i = 0; i < XWIN; ++i) X[i] = ld4<SC1>(rw, xo + (i & 3) * 1024, xb + (i >> 2) * 4096);
                aK = f32x16{}; aV = f32x16{};
                u32x4 wk[3], wv[3];
#pragma unroll
                for (int i = 0; i < 2; ++i) { wk[i] = lds_ld<u32x4>(aW1 + i * 1024); wv[i] = lds_ld<u32x4>(aW2 + i * 1024); }
                SB();
#pragma unroll
                for (int i = 0; i < 32; ++i) {
                    asm volatile("" ::"v"(wv[i % 3]));
                    SB();
                    aK = mfma32(wk[i % 3], X[i], aK);
                    if (i == 0) STAMP(24 + 16 * (1 - layer)); if (i == 20) STAMP(26 + 16 * (1 - layer));
                    SB();
                    if (i < 30) wk[(i + 2) % 3] = lds_ld<u32x4>(aW1 + (i + 2) * 1024);
                    if (i + XWIN < 32) X[(i + XWIN) & 31] = ld4<SC1>(rw, xo + ((i + XWIN) & 3) * 1024, xb + ((i + XWIN) >> 2) * 4096);
                    SB();
                    aV = mfma32(wv[i % 3], X[i], aV);
                    SB();
                    if (i == BS_AT && tries == 0) asm volatile("s_barrier" ::: "memory");
                    if (i < 30) wv[(i + 2) % 3] = lds_ld<u32x4>(aW2 + (i + 2) * 1024);
                    SB();
                }
                if (!__any(aK[0] != aK[0])) break;
                u32 m = 0;
#pragma unroll
                for (int i = 0; i < 32; ++i) m = max4(m, X[i]);
                if (!__any(m == SENT) || ++tries > 4096) break;
            }
            COUNT(28 + layer, tries);
            STAMP(7 + 10 * layer);
            const u32x2 ln = lds_ld<u32x2>(L0 + L_LN + (wave * 32 + rho) * 8);
            const u32x4 xm = {ln.x, ln.x, ln.x, ln.x};
            aK = mfma32(lds_ld<u32x4>(aW1 + 32 * 1024), xm, aK);
            aV = mfma32(lds_ld<u32x4>(aW2 + 32 * 1024), xm, aV);
            const float rs = fasu(ln.y);
            const u32 aI = L0 + L_IMG + (wave * 32 + rho) * IMG_RS;
            u32 phi[16];
            hedgehog(aK, rs, phi);
#pragma unroll
            for (int s = 0; s < 2; ++s)
                lds_st<u32x4>(aI + 128 + hh * 32 + s * 16, u32x4{pk(aV[8 * s] * rs, aV[8 * s + 1] * rs), pk(aV[8 * s + 2] * rs, aV[8 * s + 3] * rs),
                                                                 pk(aV[8 * s + 4] * rs, aV[8 * s + 5] * rs), pk(aV[8 * s + 6] * rs, aV[8 * s + 7] * rs)});
#pragma unroll
            for (int s = 0; s < 4; ++s) lds_st<u32x4>(aI + hh * 64 + s * 16, u32x4{phi[4 * s], phi[4 * s + 1], phi[4 * s + 2], phi[4 * s + 3]});
            STAMP(8 + 10 * layer);
            bar();
            f32x16 aQ = {};
            {
                u32x4 wq[4];
#pragma unroll
                for (int i = 0; i < 3; ++i) wq[i] = lds_ld<u32x4>(aW + i * 1024);
                SB();
#pragma unroll
                for (int i = 0; i < 33; ++i) {
                    if (i + 3 < 33) wq[(i + 3) & 3] = lds_ld<u32x4>(aW + (i + 3) * 1024);
                    aQ = mfma32(wq[i & 3], i < 32 ? X[i & 31] : xm, aQ);
                    SB();
                }
            }
            if (layer == 0) {
#pragma unroll
                for (int s = 0; s < 2; ++s) {
                    st4<SC1>(sent4, rw, xoff + s * 1024, WS_XF + MB2);
                    st4<SC1>(sent4, rw, ooff + s * 1024, WS_OF + MB2);
                    st4<SC1>(sent4, rw, doff, WS_DS + (s + 1) * DS_BUF);
                }
            }
            SB();
            hedgehog(aQ, rs, phi);
#pragma unroll
            for (int i = 0; i < 16; ++i) asm volatile("" : "+v"(phi[i]));
            drain();
            u32 aN = ((rho >> 2) & 1) == hh ? L0 + L_KVT + (hh * 16 + (rho & 3) + 4 * (rho >> 3)) * KVT_RS : L0 + L_ZERO;
            u32 aZ = (l16 >> 2) == g16 ? L0 + L_Z + (g16 >> 1) * 64 : L0 + L_ZERO;
            u32 oo = lane * 16 + WS_OF + ((layer * 64 + T) * 32 + 2 * rank) * 1024;
            asm volatile("" : "+v"(aN), "+v"(aZ), "+v"(oo));
            STAMP(9 + 10 * layer);
            bar();
            STAMP(10 + 10 * layer);
            {
                f32x16 num = {}; f32x4 den = {};
#pragma unroll
                for (int s = 0; s < 4; ++s) den = mfma16(lds_ld<u32x4>(aZ + s * 16), u32x4{phi[4 * s], phi[4 * s + 1], phi[4 * s + 2], phi[4 * s + 3]}, den);
#pragma unroll
                for (int s = 0; s < 4; ++s) num = mfma32(lds_ld<u32x4>(aN + s * 16), u32x4{phi[4 * s], phi[4 * s + 1], phi[4 * s + 2], phi[4 * s + 3]}, num);
                const float r = __builtin_amdgcn_rcpf(den[0] + 1e-6f);
#pragma unroll
                for (int s = 0; s < 2; ++s)
                    st4<SC1>(nos4(u32x4{pk(num[8 * s] * r, num[8 * s + 1] * r), pk(num[8 * s + 2] * r, num[8 * s + 3] * r),
                                        pk(num[8 * s + 4] * r, num[8 * s + 5] * r), pk(num[8 * s + 6] * r, num[8 * s + 7] * r)}), rw, oo + s * 1024, 0);
            }
            STAMP(11 + 10 * layer);
            bar();
            delay<DLY_O>();
            {
                u32x4 o[4];
                const u32 sp = gather4<4, 2, 1024>(o, rw, lane * 16, WS_OF + ((layer * 64 + grp * 4 + (rank >> 2)) * 32 + wave * 4) * 1024);
                COUNT(30 + layer, sp);
#pragma unroll
                for (int i = 0; i < 4; ++i) lds_st<u32x4>(L0 + L_O + (wave * 4 + i) * 1024 + lane * 16, o[i]);
            }
            STAMP(12 + 10 * layer);
            bar();
        }
        DUMP();
        asm volatile(".Lh3_end0:");
        __builtin_amdgcn_endpgm();
    } else {
        __builtin_amdgcn_s_setprio(3);
        const u32 v = wave - 4, dt = tid - 256;
        const u32 T = Td;
        const u32 tok = T * 32 + 4 * (rho & 7) + (rho >> 3);
        const u32 roff = (tok * D + tile * 32 + hh * 8) * 2;
        u32x4 xr[2];
#pragma unroll
        for (int s = 0; s < 2; ++s) xr[s] = ld4<0>(rq, roff + s * 32, 0);
        const u32 junk = ld1<0>(mkrs((const void*)pc0, wave == 7 ? codelen : 0), slot * 1024 + (lane & 15) * 64, 0);
        const u32 dD = ld1<0>(mkrs(gD, D * 2), pair * 4, 0);
        if (dt < 180) { const u32 c = dt >= 90; lds_st<u32x4>(L0 + L_UR + c * UR_CH + 64 * 96 + (dt - c * 90) * 16, u32x4{0, 0, 0, 0}); }
        if (dt == 0) lds_st<u32>(L0 + L_CNT, 0);
        clears(0);
        phase0();
        STAMP(37);
        drain();
        st1<SC1>(tag, rw, ofl, 0);
        clears(2);
        STAMP(36);
        asm volatile("" ::"v"(junk));
        u32x4 R[32];
        u32 cp[6];
        f32x2 xf[8], b1[8], b2[8];
        {
            u32x4 kv[2]; u32 au[2], ay[2];
#pragma unroll
            for (int i = 0; i < 2; ++i) {
                const u32 t0 = 4 * (v * 128 + i * 64 + lane), i0 = 2044 - t0;
                au[i] = L0 + L_UR + (i0 >> 5) * 96 + (i0 & 31) * 2; ay[i] = L0 + L_YC + (t0 >> 5) * 72 + (t0 & 31) * 2;
            }
            f32x2 d1 = up2(dD) * 65537.f, d2 = up2(dD) * -65536.f;
            u32 selLo = ONE_LO, selHi = ONE_HI, oy = WS_YP + qoff;
            asm volatile("" : "+s"(selLo), "+s"(selHi), "+v"(au[0]), "+v"(au[1]), "+v"(ay[0]), "+v"(ay[1]), "+v"(oy), "+v"(d1), "+v"(d2));
            delay<DLY_F1>();
            { const u32 sp = wait_flags<1, 2>(rw, (pair * 4 + v) * 128 + c8 * 4, WS_F1, tag); COUNT(26, sp); }
            STAMP(4);
#pragma unroll
            for (int i = 0; i < 2; ++i) kv[i] = ld4<SC1>(rw, qoff + i * 1024, WS_KVP);
            const u32 fo = WS_FW + ((rank * 16 + grp) * 4 + v) * 128 + (l16 < 12 ? l16 : 0) * 4;
            const u32 fw = ld1<SC1>(rw, fo, 0);
            SB();
#pragma unroll
            for (int i = 0; i < 2; ++i) {
                lds_st<u32x2>(au[i], u32x2{__builtin_amdgcn_perm(kv[i].z, kv[i].w, 0x05040100u), __builtin_amdgcn_perm(kv[i].x, kv[i].y, 0x05040100u)});
                lds_st<u32x2>(au[i] + UR_CH, u32x2{__builtin_amdgcn_perm(kv[i].z, kv[i].w, 0x07060302u), __builtin_amdgcn_perm(kv[i].x, kv[i].y, 0x07060302u)});
            }
            f32x2 tD[8];
#pragma unroll
            for (int i = 0; i < 8; ++i) { const f32x2 k2 = up2(kv[i >> 2][i & 3]); tD[i] = pfma(k2, d2, k2 * d1); asm volatile("" : "+v"(tD[i])); }
            STAMP(5);
            bar();
            const u32 tur = (u32)__builtin_amdgcn_s_memrealtime();
            constexpr bool LATE3 = CY == 3 || CX == 3 || CO == 3 || CZ == 3;
            if (!LATE3) st2<SC1>(u32x2{tur, tag}, rw, dt < 4 ? WS_F3 + lane * 4096 + (b >> 2) * 64 + (b & 3) * 8 : OOB, 0);
            clears(3);
            COUNT(42, 1);
            if (!__all(fw == tag)) { wait_flags<1, 2>(rw, fo, 0, tag); COUNT(42, 0); }
            fence();
            u32x2 st0 = __builtin_amdgcn_raw_buffer_load_b64(rw, tok * 8, WS_ST0, SC1);
#pragma unroll
            for (int i = 0; i < 6; ++i) cp[i] = ld1<SC1>(rw, WS_CS + (rank * 64 + v * 4 + hh * 2) * 128 + rho * 4 + (i & 1) * 128, (i >> 1) * 2048);
#pragma unroll
            for (int i = 0; i < 8; ++i) R[8 + i] = ld4<SC1>(rw, lane * 16 + (i & 3) * 1024, WS_WQ + ((rank * 3 + 1) * 32 + v * 8 + (i & 4)) * 1024);
            if (LATE3) {
                asm volatile("s_waitcnt vmcnt(15)" ::: "memory");
                u32 old = 0;
                if (lane == 0) old = __hip_atomic_fetch_add((LAS u32*)(size_t)(L0 + L_CNT), 1u, __ATOMIC_RELAXED, __HIP_MEMORY_SCOPE_WORKGROUP);
                st2<SC1>(u32x2{tur, tag}, rw, __builtin_amdgcn_readfirstlane(old) == 3 && lane < 4 ? WS_F3 + lane * 4096 + (b >> 2) * 64 + (b & 3) * 8 : OOB, 0);
            }
            STAMP(34);
#pragma unroll
            for (int i = 0; i < 8; ++i) R[16 + i] = ld4<SC1>(rw, lane * 16 + (i & 3) * 1024, WS_WQ + ((rank * 3 + 2) * 32 + v * 8 + (i & 4)) * 1024);
#pragma unroll
            for (int i = 0; i < 8; ++i) if (QW == 1) R[i] = ld4<SC1>(rw, lane * 16 + (i & 3) * 1024, WS_WQ + ((rank * 3) * 32 + v * 8 + (i & 4)) * 1024);
            STAMP(14);
            bar();
            STAMP(15);
            u32x2 ya[2], yb[2];
#pragma unroll
            for (int i = 0; i < 2; ++i) { ya[i] = lds_ld<u32x2>(ay[i]); yb[i] = lds_ld<u32x2>(ay[i] + YC_CH); }
#pragma unroll
            for (int i = 0; i < 2; ++i) {
                u32x4 y;
#pragma unroll
                for (int e = 0; e < 4; ++e) {
                    const u32 sel = (e & 1) ? selHi : selLo;
                    y[e] = nos(pk(dot2(ya[i][e >> 1], sel, tD[4 * i + e].x), dot2(yb[i][e >> 1], sel, tD[4 * i + e].y)));
                }
                st4<SC1>(y, rw, oy + i * 1024, 0);
            }
            SB();
            STAMP(23);
            if (__any(st0.y != tag)) {
                u32 sp = 0;
                do { fence(); st0 = __builtin_amdgcn_raw_buffer_load_b64(rw, tok * 8, WS_ST0, SC1); } while (__any(st0.y != tag) && !spent(sp));
            }
            const float rs = __builtin_amdgcn_rcpf(hi(st0.x)) * 1.0000003f, mean = lo(st0.x);
#pragma unroll
            for (int i = 0; i < 8; ++i) {
                xf[i] = up2(xr[i >> 2][i & 3]);
                const f32x2 n = rb2(rb2(xf[i] - mean) * rs);
                b1[i] = n * 65537.f; b2[i] = n * -65536.f;
                asm volatile("" : "+v"(xf[i]), "+v"(b1[i]), "+v"(b2[i]));
            }
        }
        STAMP(25);
        bar();
        u32x4 yt[2];
        const u32 poff = ((tile * 16 + (lane >> 3)) * 2048 + T * 32 + 4 * (lane & 7)) * 4;
#pragma unroll
        for (int s = 0; s < 2; ++s) yt[s] = ld4<SC1>(rw, poff, WS_YP + s * 65536);
        SB();
#pragma unroll
        for (int i = QW == 1 ? 0 : 8; i < 24; ++i) lds_st<u32x4>(L0 + L_W + ((i >> 3) * 33 + v * 8 + (i & 7)) * 1024 + lane * 16, R[i]);
        SB();
        { u32 sp = 0; if (__any(max4(max4(0, yt[0]), yt[1]) == SENT)) sp = 1 + gather4<2, 2, 65536>(yt, rw, poff, WS_YP); COUNT(41, sp); }
        STAMP(13);
#pragma unroll
        for (int s = 0; s < 2; ++s) yt[s] = t1(yt[s]);
#pragma unroll
        for (int i = 0; i < 8; ++i) { const f32x2 y = up2(yt[i >> 2][i & 3]); xr[i >> 2][i & 3] = nos(pk2(xf[i] + pfma(y, b2[i], y * b1[i]))); }
#pragma unroll
        for (int s = 0; s < 2; ++s) st4<SC1>(xr[s], rw, xoff + s * 1024, WS_XF);
        SB();
        STAMP(3);
        bar();
        stats_publish(xr, rw, lane, WS_DS + (T * 16 + tile) * 256);
#pragma unroll
        for (int p = 0; p < 3; ++p) {
            const float cs = halfsum(fasu(cp[2 * p]) + fasu(cp[2 * p + 1]));
            const float c0 = rb(cs), c1 = rb(cs - c0), c2 = cs - c0 - c1;
            lds_st<u32x2>(L0 + L_W + (p * 33 + 32) * 1024 + ((v >> 1) * 32 + rho) * 16 + (v & 1) * 8, u32x2{pk(c0, c1), pk(c2, 0.f)});
        }
        if (dt < 4) lds_st<u32x4>(L0 + L_ZERO + lane * 16, u32x4{0, 0, 0, 0});
        u32 aO = L0 + L_O + lane * 16;
        asm volatile("" : "+v"(aO));
        auto ln_to_lds = [&](u32 buf) __attribute__((always_inline)) {
            float mean, rs;
            const u32 sp = stats_gather(rw, lane, WS_DS + buf * DS_BUF + (grp * 4 + v) * 4096, mean, rs);
            COUNT(21, sp);
            if (lane < 32) lds_st<u32x2>(L0 + L_LN + (v * 32 + lane) * 8, u32x2{pk(-mean, -mean), uasf(rs)});
        };
        delay<(DLY_SC > 5 ? DLY_SC - 5 : 0)>();
        ln_to_lds(0);
        STAMP(7);
        bar();
#pragma unroll
        for (int i = 0; i < 32; ++i) R[i] = ld4<SC1>(rw, lane * 16 + (i & 3) * 1024, WS_WO + (tile * 32 + (i & ~3)) * 1024);
        STAMP(35);
#pragma unroll 1
        for (int layer = 0; layer < 2; ++layer) {
            bar();
            STAMP(6 + 10 * layer);
            const u32 head = v >> 1, fh = v & 1;
            const u32 spb = WS_SP + (layer * 16 + rank) * 16 * SP_UNIT;
            {
                const u32 a = L0 + L_IMG + (4 * g16 + (l16 >> 2)) * IMG_RS + (l16 & 3) * 8;
                f32x4 kvp = {}, zp = {};
                const u32x4 ones = {ONE2, ONE2, ONE2, ONE2};
#pragma unroll
                for (int ks = 0; ks < 4; ++ks) {
                    const u32 r0 = a + ks * 32 * IMG_RS, r1 = r0 + 16 * IMG_RS;
                    const u32x2 p0 = lds_tr(r0 + head * 64 + fh * 32), p1 = lds_tr(r1 + head * 64 + fh * 32);
                    const u32x2 v0 = lds_tr(r0 + 128 + head * 32), v1 = lds_tr(r1 + 128 + head * 32);
                    const u32x4 pa = {p0.x, p0.y, p1.x, p1.y};
                    kvp = mfma16(pa, u32x4{v0.x, v0.y, v1.x, v1.y}, kvp);
                    zp = mfma16(pa, ones, zp);
                }
                st2<SC1>(u32x2{nos(pk(kvp[0], kvp[1])), nos(pk(kvp[2], kvp[3]))}, rw, v * 512 + l16 * 32 + g16 * 8, spb + grp * SP_UNIT);
                st4<SC1>(nos4(__builtin_bit_cast(u32x4, zp)), rw, l16 == 0 ? 2048 + v * 64 + g16 * 16 : OOB, spb + grp * SP_UNIT);
            }
            STAMP(layer ? 39 : 38);
            delay<DLY_ST>();
            {
                u32x4 pt[8]; u32 zt[4]; u32 sp = 0;
                while (true) {
                    fence();
#pragma unroll
                    for (int i = 0; i < 8; ++i) pt[i] = ld4<SC1>(rw, (g16 >> 1) * SP_UNIT + v * 512 + l16 * 32 + (g16 & 1) * 16, spb + 2 * i * SP_UNIT);
#pragma unroll
                    for (int i = 0; i < 4; ++i) zt[i] = ld1<SC1>(rw, g16 * SP_UNIT + 2048 + v * 64 + l16 * 4, spb + 4 * i * SP_UNIT);
                    u32 m = 0;
#pragma unroll
                    for (int i = 0; i < 8; ++i) m = max4(m, pt[i]);
                    m = max(max(m, max(zt[0], zt[1])), max(zt[2], zt[3]));
                    if (!__any(m == SENT) || spent(sp)) break;
                    delay<2>();
                }
                COUNT(28 + layer, sp);
                const u32 e = l16 & 7, one = (l16 >> 3) == (g16 & 1) ? ((e & 1) ? ONE_HI : ONE_LO) : 0u;
                const u32x4 idm = {(e >> 1) == 0 ? one : 0u, (e >> 1) == 1 ? one : 0u, (e >> 1) == 2 ? one : 0u, (e >> 1) == 3 ? one : 0u};
                f32x4 t = {};
#pragma unroll
                for (int i = 0; i < 8; ++i) t = mfma16(idm, pt[i], t);
                const float z = halfsum(rowpairsum((fasu(zt[0]) + fasu(zt[1])) + (fasu(zt[2]) + fasu(zt[3]))));
                lds_st<u32x2>(L0 + L_KVT + (head * 16 + l16) * KVT_RS + fh * 32 + g16 * 8, u32x2{pk(t[0], t[1]), pk(t[2], t[3])});
                if (lane < 16) lds_st<u16>(L0 + L_Z + head * 64 + fh * 32 + lane * 2, (u16)pk(z, z));
            }
            STAMP(8 + 10 * layer);
            bar();
            bar();
            delay<DLY_O>();
            {
                u32x4 o[4];
                const u32 sp = gather4<4, 2, 1024>(o, rw, lane * 16, WS_OF + ((layer * 64 + T) * 32 + wave * 4) * 1024);
                COUNT(30 + layer, sp);
#pragma unroll
                for (int i = 0; i < 4; ++i) lds_st<u32x4>(L0 + L_O + (wave * 4 + i) * 1024 + lane * 16, o[i]);
            }
            bar();
            STAMP(9 + 10 * layer);
            f32x16 acc;
#pragma unroll
            for (int i = 0; i < 8; ++i) { acc[2 * i] = lo(xr[i >> 2][i & 3]); acc[2 * i + 1] = hi(xr[i >> 2][i & 3]); }
            {
                u32x4 of[4];
#pragma unroll
                for (int i = 0; i < 3; ++i) of[i] = lds_ld<u32x4>(aO + i * 1024);
                SB();
#pragma unroll
                for (int i = 0; i < 32; ++i) {
                    if (i + 3 < 32) of[(i + 3) & 3] = lds_ld<u32x4>(aO + (i + 3) * 1024);
                    acc = mfma32(R[i], of[i & 3], acc);
                    SB();
                }
            }
#pragma unroll
            for (int i = 0; i < 8; ++i) xr[i >> 2][i & 3] = pk(acc[2 * i], acc[2 * i + 1]);
            STAMP(10 + 10 * layer);
            if (layer == 1) {
                const rsrc_t ro = mkrs(gout, S * D * 2);
                st4<0>(xr[0], ro, roff, 0);
                st4<0>(xr[1], ro, roff + 32, 0);
                STAMP(22);
                break;
            }
            stats_publish(xr, rw, lane, WS_DS + DS_BUF + (T * 16 + tile) * 256);
            asm volatile("" : "+v"(yt[0]), "+v"(yt[1]));
#pragma unroll
            for (int i = 0; i < 8; ++i) {
                xf[i] = up2(xr[i >> 2][i & 3]);
                const f32x2 y = up2(yt[i >> 2][i & 3]);
                b1[i] = y * 65537.f; b2[i] = y * -65536.f;
                asm volatile("" : "+v"(xf[i]), "+v"(b1[i]), "+v"(b2[i]));
            }
            delay<DLY_DS>();
            float mean, rs;
            { const u32 sp = stats_gather(rw, lane, WS_DS + DS_BUF + T * 4096, mean, rs); COUNT(24, sp); }
            STAMP(11);
#pragma unroll
            for (int i = 0; i < 8; ++i) {
                const f32x2 n = rb2(rb2(xf[i] - mean) * rs);
                xr[i >> 2][i & 3] = nos(pk2(xf[i] + pfma(n, b2[i], n * b1[i])));
            }
#pragma unroll
            for (int s = 0; s < 2; ++s) st4<SC1>(xr[s], rw, xoff + s * 1024, WS_XF + MB2);
            SB();
            STAMP(12);
            bar();
            stats_publish(xr, rw, lane, WS_DS + 2 * DS_BUF + (T * 16 + tile) * 256);
            delay<DLY_SC>();
            ln_to_lds(2);
            STAMP(17);
            bar();
        }
        if (b == 0 && tid == 256) *(u32*)(ws + WS_E) = tag;
        DUMP();
        asm volatile(".Lh3_end1:");
        __builtin_amdgcn_endpgm();
    }
}

extern "C" void kernel_launch(void* const* d_in, const int* in_sizes, int n_in, void* d_out, int out_size, void* d_ws, size_t ws_size, hipStream_t stream) {
    (void)hipFuncSetAttribute((const void*)h3_hedgehog_kernel, hipFuncAttributeMaxDynamicSharedMemorySize, LDS_BYTES);
    h3_hedgehog_kernel<<<256, 512, LDS_BYTES, stream>>>((const u16*)d_in[0], (const u16*)d_in[1], (const u16*)d_in[2], (const u16*)d_in[3], (const u16*)d_in[4],
                                                        (const u16*)d_in[5], (const u16*)d_in[7], (const u16*)d_in[8], (const u16*)d_in[9], (u16*)d_out, (char*)d_ws);
}
```

```cpp
#include <hip/hip_runtime.h>
#include <stdint.h>

typedef unsigned u32;
typedef unsigned short u16;
typedef unsigned long long u64;
typedef u32 u32x4 __attribute__((ext_vector_type(4)));
typedef u32 u32x2 __attribute__((ext_vector_type(2)));
typedef float f32x2 __attribute__((ext_vector_type(2)));
typedef float f32x4 __attribute__((ext_vector_type(4)));
typedef float f32x16 __attribute__((ext_vector_type(16)));
typedef __bf16 bf16x2 __attribute__((ext_vector_type(2)));
typedef __bf16 bf16x8 __attribute__((ext_vector_type(8)));
typedef short s16x4 __attribute__((ext_vector_type(4)));
typedef __amdgpu_buffer_rsrc_t rsrc_t;
#define LAS __attribute__((address_space(3)))
#define DI __device__ __forceinline__

#define DLY_F1 14
#define DLY_S0 0
#define DLY_SC 10
#define XWIN 12
#define MAP48 0
#define H1SAME 1
#define WO_EARLY 12
#define Y_WAIT 45
#define H_LATE 0
#define DLY_ST 10
#define DLY_O 12
#define DLY_DS 8
#define DLY_X 12
#define CY 1
#define CX 2
#define CO 4
#define CZ 2
#define QW 2
#define BS_AT 20

constexpr int S = 2048, D = 512;
constexpr u32 MB2 = 2u << 20;
constexpr u32 WS_WQ = 0, WS_WO = 0x180000, WS_KVP = MB2, WS_YP = 3 * MB2, WS_XF = 4 * MB2,
              WS_OF = 6 * MB2, WS_SP = 8 * MB2, SP_UNIT = 2304, WS_DS = WS_SP + 2 * 16 * 16 * SP_UNIT, DS_BUF = 64 * 16 * 256,
              WS_STAMP = 9 * MB2, WS_CTRL = WS_STAMP + 0x100000, WS_E = WS_CTRL, WS_F1 = WS_CTRL + 4096, WS_FW = WS_F1 + 256 * 512, WS_F3 = WS_FW + 256 * 512,
              WS_ST0 = WS_F3 + 4 * 4096, WS_CS = WS_ST0 + 16384, WS_BYTES = 10 * MB2;
static_assert(WS_DS + 3 * DS_BUF <= WS_STAMP, "ws");
constexpr u32 OOB = 0x80000000u;
constexpr u32 L_W = 0, L_R2 = 3 * 33 * 1024;
constexpr u32 L_HC = L_R2, HC_COPY = 4160, HC_CH = 4 * HC_COPY, L_UR = L_HC + 2 * HC_CH, UR_CH = 79 * 96, L_YC = L_UR + 2 * UR_CH, YC_CH = 64 * 72;
constexpr u32 L_PK = 0  , L_PV = 72 * 128, L_PX = L_PV + 64 * 128;
constexpr u32 L_IMG = L_R2, IMG_RS = 224, L_O = L_R2, L_KVT = L_R2 + 32768, KVT_RS = 80, L_Z = L_KVT + 32 * KVT_RS, L_ZERO = L_Z + 128, L_LN = L_ZERO + 64;
constexpr u32 LDS_BYTES = 163840, L_CNT = LDS_BYTES - 16;
static_assert(L_YC + 2 * YC_CH <= LDS_BYTES && L_ZERO + 64 <= LDS_BYTES, "lds");
constexpr int SC1 = 16;
constexpr u32 SENT = 0xFFFFFFFFu;
constexpr u32 SPIN_CAP = 1u << 15;

DI u32 pk(float a, float b) { return __builtin_bit_cast(u32, __builtin_convertvector(f32x2{a, b}, bf16x2)); }
DI float lo(u32 x) { return __builtin_bit_cast(float, x << 16); }
DI float hi(u32 x) { return __builtin_bit_cast(float, x & 0xFFFF0000u); }
DI float rb(float x) { return lo(pk(x, x)); }
DI float dot2(u32 a, u32 b, float c) { return __builtin_amdgcn_fdot2_f32_bf16(__builtin_bit_cast(bf16x2, a), __builtin_bit_cast(bf16x2, b), c, false); }
DI f32x2 pfma(f32x2 a, f32x2 b, f32x2 c) { return __builtin_elementwise_fma(a, b, c); }
DI f32x2 rb2(f32x2 x) { return pfma(x, f32x2{-65536.f, -65536.f}, x * 65537.f); }
DI f32x2 up2(u32 x) { return f32x2{lo(x), hi(x)}; }
DI u32 pk2(f32x2 x) { return pk(x.x, x.y); }
DI f32x16 mfma32(u32x4 a, u32x4 b, f32x16 c) { return __builtin_amdgcn_mfma_f32_32x32x16_bf16(__builtin_bit_cast(bf16x8, a), __builtin_bit_cast(bf16x8, b), c, 0, 0, 0); }
DI f32x4 mfma16(u32x4 a, u32x4 b, f32x4 c) { return __builtin_amdgcn_mfma_f32_16x16x32_bf16(__builtin_bit_cast(bf16x8, a), __builtin_bit_cast(bf16x8, b), c, 0, 0, 0); }
DI rsrc_t mkrs(const void* p, u32 bytes) { return __builtin_amdgcn_make_buffer_rsrc((void*)p, 0, bytes, 0x00020000); }
template <int AUX> DI u32x4 ld4(rsrc_t r, u32 v, u32 s) { return __builtin_amdgcn_raw_buffer_load_b128(r, v, s, AUX); }
template <int AUX> DI u32 ld1(rsrc_t r, u32 v, u32 s) { return __builtin_amdgcn_raw_buffer_load_b32(r, v, s, AUX); }
template <int AUX> DI void st4(u32x4 x, rsrc_t r, u32 v, u32 s) { __builtin_amdgcn_raw_buffer_store_b128(x, r, v + s, 0, AUX); }
template <int AUX> DI void st2(u32x2 x, rsrc_t r, u32 v, u32 s) { __builtin_amdgcn_raw_buffer_store_b64(x, r, v, s, AUX); }
template <int AUX> DI void st1(u32 x, rsrc_t r, u32 v, u32 s) { __builtin_amdgcn_raw_buffer_store_b32(x, r, v, s, AUX); }
DI void dma(rsrc_t r, u32 v, u32 s, u32 ldsaddr) {
    asm volatile("s_nop 4\n\ts_mov_b32 m0, %0\n\ts_nop 0\n\tbuffer_load_dwordx4 %1, %2, %3 offen lds" ::"s"(ldsaddr), "v"(v), "s"(r), "s"(s) : "memory");
}
template <typename T> DI T lds_ld(u32 a) { return *(LAS T*)(size_t)a; }
template <typename T> DI void lds_st(u32 a, T v) { *(LAS T*)(size_t)a = v; }
DI u32x2 lds_tr(u32 a) { return __builtin_bit_cast(u32x2, __builtin_amdgcn_ds_read_tr16_b64_v4i16((LAS s16x4*)(size_t)a)); }
DI void bar() { asm volatile("s_waitcnt lgkmcnt(0)\n\ts_barrier" ::: "memory"); }
DI void drain() { asm volatile("s_waitcnt vmcnt(0)" ::: "memory"); }
DI void fence() { asm volatile("" ::: "memory"); }
DI bool spent(u32& sp) { u32 one; asm volatile("s_mov_b32 %0, 1" : "=s"(one)); sp += one; return sp > SPIN_CAP; }
template <int N> DI void delay() { if (N > 0) __builtin_amdgcn_s_sleep(N); }
DI u32 nos(u32 x) { return x < 0xFFFFFFFEu ? x : 0xFFFFFFFEu; }
DI u32x4 nos4(u32x4 x) { return u32x4{nos(x.x), nos(x.y), nos(x.z), nos(x.w)}; }
DI u32 max4(u32 m, u32x4 v) { return max(max(max(max(m, v.x), v.y), v.z), v.w); }
DI float fasu(u32 x) { return __builtin_bit_cast(float, x); }
DI u32 uasf(float x) { return __builtin_bit_cast(u32, x); }
DI float halfsum(float x) { auto r = __builtin_amdgcn_permlane32_swap(uasf(x), uasf(x), false, false); return fasu(r[0]) + fasu(r[1]); }
DI float rowpairsum(float x) { auto r = __builtin_amdgcn_permlane16_swap(uasf(x), uasf(x), false, false); return fasu(r[0]) + fasu(r[1]); }
template <int CTRL> DI float dppadd(float x) { return x + fasu(__builtin_amdgcn_update_dpp(0u, uasf(x), CTRL, 0xf, 0xf, true)); }
DI u32x4 t1(u32x4 a) {
    auto r02 = __builtin_amdgcn_permlane16_swap(a.x, a.z, false, false);
    auto r13 = __builtin_amdgcn_permlane16_swap(a.y, a.w, false, false);
    u32 a0 = r02[0], a2 = r02[1], a1 = r13[0], a3 = r13[1];
    u32x4 o;
    o.x = __builtin_amdgcn_update_dpp(a0, a1, 0x128, 0xf, 0xc, false);
    o.y = __builtin_amdgcn_update_dpp(a1, a0, 0x128, 0xf, 0x3, false);
    o.z = __builtin_amdgcn_update_dpp(a2, a3, 0x128, 0xf, 0xc, false);
    o.w = __builtin_amdgcn_update_dpp(a3, a2, 0x128, 0xf, 0x3, false);
    return o;
}
#define SB() __builtin_amdgcn_sched_barrier(0)
DI void lnstat(float sum, float sq, float& m, float& s) {
    float mean = sum * (1.f / D);
    float var = fmaxf(sq * (1.f / D) - mean * mean, 0.f);
    m = rb(mean);
    s = rb(__builtin_amdgcn_sqrtf(rb(rb(var) + 1e-5f)));
}
constexpr u32 ONE_LO = 0x00003F80u, ONE_HI = 0x3F800000u, ONE2 = 0x3F803F80u;

DI void softmax16(const f32x16& p, float c, float a, u32* o) {
    f32x2 e[8];
    const f32x2 c2 = {c, c}, a2 = {a, a};
#pragma unroll
    for (int i = 0; i < 8; ++i) {
        const f32x2 t = __builtin_elementwise_fma(f32x2{p[2 * i], p[2 * i + 1]}, c2, a2);
        e[i] = f32x2{__builtin_amdgcn_exp2f(t.x), __builtin_amdgcn_exp2f(t.y)};
    }
    const f32x2 s = ((e[0] + e[1]) + (e[2] + e[3])) + ((e[4] + e[5]) + (e[6] + e[7]));
    const float r = __builtin_amdgcn_rcpf(s.x + s.y);
#pragma unroll
    for (int i = 0; i < 8; ++i) { const f32x2 t = e[i] * r; o[i] = pk(t.x, t.y); }
}
DI void hedgehog(const f32x16& p, float rs, u32 (&o)[16]) {
    const float mx = fmaxf(fmaxf(fmaxf(fmaxf(p[0], p[1]), p[2]), fmaxf(fmaxf(p[3], p[4]), p[5])), fmaxf(fmaxf(fmaxf(fmaxf(p[6], p[7]), p[8]), fmaxf(fmaxf(p[9], p[10]), p[11])), fmaxf(fmaxf(fmaxf(p[12], p[13]), p[14]), p[15])));
    const float mn = fminf(fminf(fminf(fminf(p[0], p[1]), p[2]), fminf(fminf(p[3], p[4]), p[5])), fminf(fminf(fminf(fminf(p[6], p[7]), p[8]), fminf(fminf(p[9], p[10]), p[11])), fminf(fminf(fminf(p[12], p[13]), p[14]), p[15])));
    const float c = rs * 1.44269504088896f;
    softmax16(p, c, -mx * c, &o[0]);
    softmax16(p, -c, mn * c, &o[8]);
}

#define STAMP(i) do { if ((i) == 0) tprobe = (u32)__builtin_amdgcn_s_memrealtime(); } while (0)
#define COUNT(i, v)
#define DUMP() do { if (wave == 4 && (__builtin_amdgcn_s_getreg((3 << 11) | 20) & 0xF) == 5) { const u32 te = (u32)__builtin_amdgcn_s_memrealtime(); while ((u32)__builtin_amdgcn_s_memrealtime() - te < tprobe - tstart) __builtin_amdgcn_s_sleep(1); } } while (0)

template <int N, int BACKOFF> DI u32 wait_flags(rsrc_t r, u32 v, u32 s, u32 tag) {
    u32 sp = 0;
#pragma nounroll
    while (true) {
        fence();
        bool ok;
        if (N == 1) ok = ld1<SC1>(r, v, s) == tag;
        else { const u32x4 f = ld4<SC1>(r, v, s); ok = f.x == tag && f.y == tag && f.z == tag && f.w == tag; }
        if (__all(ok) || spent(sp)) break;
        delay<BACKOFF>();
    }
    fence();
    return sp;
}
template <int N, int BACKOFF, int STRIDE> DI u32 gather4(u32x4 (&o)[N], rsrc_t r, u32 v, u32 s) {
    u32 sp = 0;
#pragma nounroll
    while (true) {
        asm volatile("" : "+v"(v) :: "memory");
        u32 m = 0;
#pragma unroll
        for (int i = 0; i < N; ++i) o[i] = STRIDE < 4096 ? ld4<SC1>(r, v + i * STRIDE, s) : ld4<SC1>(r, v, s + i * STRIDE);
#pragma unroll
        for (int i = 0; i < N; ++i) m = max4(m, o[i]);
        if (!__any(m == SENT) || spent(sp)) break;
        delay<BACKOFF>();
    }
    return sp;
}
DI void stats_publish(const u32x4 (&x)[2], rsrc_t r, u32 lane, u32 off) {
    float s0 = 0.f, s1 = 0.f, q0 = 0.f, q1 = 0.f;
#pragma unroll
    for (int i = 0; i < 4; ++i) { s0 = dot2(x[0][i], ONE2, s0); q0 = dot2(x[0][i], x[0][i], q0); s1 = dot2(x[1][i], ONE2, s1); q1 = dot2(x[1][i], x[1][i], q1); }
    auto p = __builtin_amdgcn_permlane32_swap(uasf(s0 + s1), uasf(q0 + q1), false, false);
    st1<SC1>(nos(uasf(fasu(p[0]) + fasu(p[1]))), r, (lane & 15) * 16 + (lane >> 4) * 4, off);
}
DI u32 stats_gather(rsrc_t r, u32 lane, u32 off, float& mean, float& rs) {
    u32x4 ds[4];
    const u32 sp = gather4<4, 2, 1024>(ds, r, lane * 16, off);
    const f32x4 a = (__builtin_bit_cast(f32x4, ds[0]) + __builtin_bit_cast(f32x4, ds[1])) + (__builtin_bit_cast(f32x4, ds[2]) + __builtin_bit_cast(f32x4, ds[3]));
    auto p0 = __builtin_amdgcn_permlane32_swap(uasf(a[0]), uasf(a[2]), false, false);
    auto p1 = __builtin_amdgcn_permlane32_swap(uasf(a[1]), uasf(a[3]), false, false);
    auto p2 = __builtin_amdgcn_permlane16_swap(uasf(fasu(p0[0]) + fasu(p0[1])), uasf(fasu(p1[0]) + fasu(p1[1])), false, false);
    const float t = fasu(p2[0]) + fasu(p2[1]);
    auto p = __builtin_amdgcn_permlane32_swap(uasf(t), uasf(t), false, false);
    float sd; lnstat(fasu(p[0]), fasu(p[1]), mean, sd);
    rs = __builtin_amdgcn_rcpf(sd) * 1.0000003f;
    return sp;
}

__global__ void __launch_bounds__(512, 2) h3_hedgehog_kernel(const u16* gq, const u16* gk, const u16* gv, const u16* gsk, const u16* gh, const u16* gD,
                                                             const u16* gW, const u16* gphi, const u16* gwo, u16* gout, char* ws) {
    u64 ts_; asm volatile("s_memrealtime %0\n\ts_waitcnt lgkmcnt(0)" : "=s"(ts_) :: "memory");
    const u32 tstart = (u32)ts_; u32 tprobe = tstart;
    extern __shared__ __attribute__((aligned(16))) char smem[];
    const u32 tid = threadIdx.x, lane = tid & 63, wave = __builtin_amdgcn_readfirstlane(tid >> 6), b = blockIdx.x;
    const u32 xcd = b & 7, slot = b >> 3;
    const u32 L0 = (u32)(size_t)(LAS char*)smem;
    asm volatile("" ::"s"(gq), "s"(gk), "s"(gv), "s"(gsk), "s"(gh), "s"(gD), "s"(gW), "s"(gphi), "s"(gwo), "s"(gout), "s"(ws));
    u64 pc0; u32 codelen;
    asm volatile("s_getpc_b64 %0\n.Lh3_here:\n\ts_mov_b32 %1, .Lh3_end0-.Lh3_here\n\ts_max_i32 %1, %1, .Lh3_end1-.Lh3_here\n\ts_max_i32 %1, %1, 0" : "=s"(pc0), "=s"(codelen) :: "scc");
    const rsrc_t rw = mkrs(ws, WS_BYTES);
    STAMP(0);
    const u32 tt = H1SAME ? slot : xcd * 4 + (slot >> 3), cg = H1SAME ? xcd : slot & 7;
    const u32 r8 = lane >> 3, c8 = lane & 7;
    {
        const rsrc_t rk = mkrs(gk, S * D * 2), rv = mkrs(gv, S * D * 2);
        const u32 row = wave * 8 + r8;
        const u32 g0 = (tt * 64 + row) * 1024 + cg * 128;
        dma(rk, g0 + ((c8 ^ (((row + 8) >> 2) & 7)) << 4), 0, L0 + L_PK + (wave + 1) * 1024);
        dma(rv, g0 + ((c8 ^ ((row >> 2) & 7)) << 4), 0, L0 + L_PV + wave * 1024);
        if (wave == 0) dma(rk, tt == 0 ? OOB : g0 - 8192 + ((c8 ^ ((r8 >> 2) & 7)) << 4), 0, L0 + L_PK);
    }
    const u32 pp = tid >> 4, tq = tid & 15;
    const u32x4 wsk = ld4<0>(mkrs(gsk, D * 4 * 2), (cg * 32 + pp) * 16, 0);
    const u32 tagv = ld1<0>(rw, 0, WS_E);
    fence();
    const rsrc_t rq = mkrs(gq, S * D * 2);
    const u32 pair = xcd * 32 + slot;
    const u32 grp = MAP48 ? (xcd >> 1) * 4 + (slot & 3) : xcd * 2 + (slot & 1);
    const u32 rank = MAP48 ? (xcd & 1) * 8 + (slot >> 2) : slot >> 1;
    const u32 rho = lane & 31, hh = lane >> 5, g16 = lane >> 4, l16 = lane & 15;
    const u32 w4 = wave & 3, Td = grp * 4 + (rank >> 2), tile = (rank & 3) * 4 + w4;
    const u32x4 sent4 = {SENT, SENT, SENT, SENT};
    const u32 qoff = pair * 8192 + w4 * 2048 + lane * 16;
    const u32 doff = lane < 16 ? (Td * 16 + tile) * 256 + lane * 16 : OOB;
    const u32 ooff = ((grp * 4 + w4) * 32 + 2 * rank) * 1024 + lane * 16;
    const u32 xoff = (Td * 32 + 2 * tile) * 1024 + lane * 16;
    auto clears = [&](int at) __attribute__((always_inline)) {
#pragma unroll
        for (int i = 0; i < 2; ++i) {
            if (CY == at) st4<SC1>(sent4, rw, qoff + i * 1024, WS_YP);
            if (CX == at) st4<SC1>(sent4, rw, xoff + i * 1024, WS_XF);
            if (CO == at) st4<SC1>(sent4, rw, ooff + i * 1024, WS_OF);
        }
        if (CZ == at) {
            const u32 soff = lane < 36 ? (rank * 16 + grp) * SP_UNIT + (lane < 32 ? w4 * 512 + lane * 16 : 2048 + w4 * 64 + (lane - 32) * 16) : OOB;
            st4<SC1>(sent4, rw, soff, WS_SP);
            st4<SC1>(sent4, rw, soff, WS_SP + 256 * SP_UNIT);
            st4<SC1>(sent4, rw, doff, WS_DS);
        }
        fence();
    };
    u32 tag, ofl;
    auto phase0 = [&]() __attribute__((always_inline)) {
        const u32 cw = (pp & 3) * 4, pch = pp >> 2;
        u32 a1 = L0 + L_PK + (4 * tq + 4) * 128 + ((pch ^ ((tq + 1) & 7)) << 4) + cw;
        u32 a2 = L0 + L_PK + (4 * tq + 8) * 128 + ((pch ^ ((tq + 2) & 7)) << 4) + cw;
        u32 a3 = L0 + L_PV + (4 * tq) * 128 + ((pch ^ (tq & 7)) << 4) + cw;
        u32 okv = WS_KVP + (cg * 32 + pp) * 8192 + (tt * 64 + 4 * tq) * 4;
        ofl = lane < 4 ? WS_F1 + ((cg * 32 + wave * 4 + lane) * 4 + (tt >> 3)) * 128 + (tt & 7) * 4 : OOB;
        f32x2 w1[4], w2[4];
#pragma unroll
        for (int j = 0; j < 4; ++j) {
            const f32x2 w = (j & 1) ? f32x2{hi(wsk[j >> 1]), hi(wsk[2 + (j >> 1)])} : f32x2{lo(wsk[j >> 1]), lo(wsk[2 + (j >> 1)])};
            w1[j] = w * 65537.f; w2[j] = w * -65536.f;
            asm volatile("" : "+v"(w1[j]), "+v"(w2[j]));
        }
        asm volatile("" : "+v"(a1), "+v"(a2), "+v"(a3), "+v"(okv), "+v"(ofl));
        STAMP(1);
        bar();
        if (wave >= 4) clears(1);
        u32 kk[7], vv[4];
#pragma unroll
        for (int i = 0; i < 3; ++i) kk[i] = lds_ld<u32>(a1 + (i + 1) * 128);
#pragma unroll
        for (int i = 0; i < 4; ++i) kk[3 + i] = lds_ld<u32>(a2 + i * 128);
#pragma unroll
        for (int i = 0; i < 4; ++i) vv[i] = lds_ld<u32>(a3 + i * 128);
        f32x2 kf[7], sm[4];
#pragma unroll
        for (int i = 0; i < 7; ++i) kf[i] = up2(kk[i]);
#pragma unroll
        for (int i = 0; i < 4; ++i) sm[i] = pfma(kf[i], w2[3], kf[i] * w1[3]);
#pragma unroll
        for (int j = 2; j >= 0; --j)
#pragma unroll
            for (int i = 0; i < 4; ++i) sm[i] = rb2(sm[i] + pfma(kf[i + 3 - j], w2[j], kf[i + 3 - j] * w1[j]));
        u32x4 kv;
#pragma unroll
        for (int i = 0; i < 4; ++i) kv[i] = pk2(sm[i] * up2(vv[i]));
        st4<SC1>(kv, rw, okv, 0);
        tag = __builtin_amdgcn_readfirstlane(tagv) + 1u;
        STAMP(2);
    };

    if (wave < 4) {
        u32x4 xrow[2], hA[2], wa0, wa1; u32x2 hB[2]; u32 ph[8];
        const rsrc_t rh = mkrs(gh + (size_t)pair * 2 * S, 2 * S * 2);
        const u32 srow = grp * 128 + rank * 8 + wave * 2 + hh;
        {
            const bool isW = wave < 3;
            const u32 krow = isW ? (rho & 0x13) | ((rho & 4) << 1) | ((rho & 8) >> 1) : (rho & 3) | ((rho & 4) << 2) | ((rho & 24) >> 1);
            const rsrc_t r = mkrs(isW ? gW : gwo, isW ? D * 3 * D * 2 : D * D * 2);
            const u32 o = (grp * 32 + krow) * (isW ? 3072 : 1024) + (isW ? wave * 1024 : 0) + rank * 64 + hh * 16;
            wa0 = ld4<0>(r, o, 0); wa1 = ld4<0>(r, o + 32, 0);
            const rsrc_t rp = mkrs(gphi, wave < 2 ? 32 * 256 * 2 : 0);
            const u32 head = rank * 2 + ((rho >> 2) & 1), f = (rho & 3) + 4 * (rho >> 3);
#pragma unroll
            for (int e = 0; e < 8; ++e) ph[e] = __builtin_amdgcn_raw_buffer_load_b16(rp, (head * 256 + hh * 128 + f) * 2 + e * 32, 0, 0);
#pragma unroll
            for (int i = 0; i < 2; ++i) xrow[i] = ld4<0>(rq, srow * 1024 + i * 512 + rho * 16, 0);
        }
        auto hload = [&]() __attribute__((always_inline)) {
#pragma unroll
            for (int i = 0; i < 2; ++i) {
                hA[i] = ld4<0>(rh, i * 4096 + tid * 16, 0);
                hB[i] = __builtin_amdgcn_raw_buffer_load_b64(rh, tid == 255 ? OOB : i * 4096 + tid * 16 + 16, 0, 0);
            }
        };
        if (H_LATE == 0) hload();
        fence();
        phase0();
        SB();
        {
            u32x4 bf; u32 sel;
            if (wave < 2) {
                sel = (rho >> 2) & 1;
                bf = u32x4{ph[0] | (ph[1] << 16), ph[2] | (ph[3] << 16), ph[4] | (ph[5] << 16), ph[6] | (ph[7] << 16)};
            } else {
                const u32 n = wave < 3 ? (((rho >> 2) & 1) * 16 + (rho & 3) + 4 * (rho >> 3)) : ((rho & 0x13) | ((rho & 4) << 1) | ((rho & 8) >> 1));
                sel = n >> 4;
                const u32 e = (n & 15) - hh * 8;
                const u32 one = (e & 1) ? ONE_HI : ONE_LO;
                bf = u32x4{(e >> 1) == 0 ? one : 0u, (e >> 1) == 1 ? one : 0u, (e >> 1) == 2 ? one : 0u, (e >> 1) == 3 ? one : 0u};
            }
            const u32x4 z4 = {0, 0, 0, 0};
            f32x16 acc = {};
            acc = mfma32(wa0, sel == 0 ? bf : z4, acc);
            acc = mfma32(wa1, sel == 1 ? bf : z4, acc);
            const u32 o = (wave < 3 ? WS_WQ + ((rank * 3 + wave) * 32 + grp * 2) * 1024 : WS_WO + (rank * 32 + grp * 2) * 1024);
            float cs = 0.f;
#pragma unroll
            for (int s = 0; s < 2; ++s) {
                const u32x4 w = {pk(acc[8 * s], acc[8 * s + 1]), pk(acc[8 * s + 2], acc[8 * s + 3]), pk(acc[8 * s + 4], acc[8 * s + 5]), pk(acc[8 * s + 6], acc[8 * s + 7])};
                st4<SC1>(w, rw, lane * 16 + s * 1024, o);
#pragma unroll
                for (int i = 0; i < 4; ++i) cs = dot2(w[i], ONE2, cs);
            }
            st1<SC1>(uasf(halfsum(cs)), rw, lane < 32 ? WS_CS + ((rank * 4 + wave) * 16 + grp) * 128 + lane * 4 : OOB, 0);
        }
        {
            float sum = 0.f, sq = 0.f;
#pragma unroll
            for (int j = 0; j < 8; ++j) { sum = dot2(xrow[j >> 2][j & 3], ONE2, sum); sq = dot2(xrow[j >> 2][j & 3], xrow[j >> 2][j & 3], sq); }
            sum = rowpairsum(dppadd<0x128>(dppadd<0x124>(dppadd<0x4E>(dppadd<0xB1>(sum)))));
            sq = rowpairsum(dppadd<0x128>(dppadd<0x124>(dppadd<0x4E>(dppadd<0xB1>(sq)))));
            float m, s; lnstat(sum, sq, m, s);
            st2<SC1>(u32x2{pk(m, s), tag}, rw, rho == 0 ? srow * 8 : OOB, WS_ST0);
        }
        STAMP(14);
        asm volatile("s_waitcnt vmcnt(4)" ::: "memory");
        STAMP(32);
        st1<SC1>(tag, rw, ofl, 0);
        asm volatile("s_waitcnt vmcnt(2)" ::: "memory");
        st1<SC1>(tag, rw, wave < 3 && lane < 16 ? WS_FW + ((rank * 16 + lane) * 4 + (grp >> 2)) * 128 + ((grp & 3) * 3 + wave) * 4 : OOB, 0);
        STAMP(3);
        if (H_LATE == 2) hload();
        {
            auto put = [&](u32 ch, u32 oct, u32x4 a, u32x2 c) {
                const u32 d[6] = {a.x, a.y, a.z, a.w, c.x, c.y};
                u32 al[5];
#pragma unroll
                for (int i = 0; i < 5; ++i) al[i] = __builtin_amdgcn_alignbit(d[i + 1], d[i], 16);
                const u32 p = L0 + L_HC + ch * HC_CH + oct * 16;
                lds_st<u32x4>(p, u32x4{al[0], al[1], al[2], al[3]});
                lds_st<u32x4>(p + HC_COPY, u32x4{d[1], d[2], d[3], d[4]});
                lds_st<u32x4>(p + 2 * HC_COPY, u32x4{al[1], al[2], al[3], al[4]});
                lds_st<u32x4>(p + 3 * HC_COPY, u32x4{d[2], d[3], d[4], d[5]});
            };
#pragma unroll 1
            for (int i = 0; i < 2; ++i) {
                const u32x4 a = i ? hA[1] : hA[0];
                put(i, tid + 4, a, i ? hB[1] : hB[0]);
                if (tid < 4) put(i, 3 - tid, u32x4{0, 0, 0, 0}, tid == 0 ? u32x2{a.x, a.y} : u32x2{0, 0});
            }
        }
        STAMP(13);
        bar();
        STAMP(4);
        const u32 tur = (u32)__builtin_amdgcn_s_memrealtime();
        {
            const u32 ch = wave >> 1, it = wave & 1;
            u32 aA = L0 + L_HC + ch * HC_CH + (l16 & 3) * HC_COPY + 2 * (16 * it + 8 * g16 + (l16 & 12));
            u32 aB = L0 + L_UR + ch * UR_CH + (15 - l16) * 96 + 16 * g16;
            f32x4 acc[4] = {};
            asm volatile(
                ".macro H3MM d, a, u\n\tv_mfma_f32_16x16x32_bf16 \\d, v[\\a:\\a+3], v[\\u:\\u+3], \\d\n.endm\n"
                ".macro H3RA r, i, n, o\n\tds_read_b64 v[\\r+4*\\i+2*\\o:\\r+4*\\i+2*\\o+1], %4 offset:1024*\\i+64*\\n+8*\\o\n.endm\n"
                ".macro H3RU r, i, n\n\tds_read_b128 v[\\r+16+4*\\i:\\r+16+4*\\i+3], %5 offset:1536*(3-\\i)+96*\\n\n.endm\n"
                ".macro H3STEP x, y, n\n"
                "s_waitcnt lgkmcnt(6)\n"
                "H3MM %0, \\x, \\x+16\n H3RA \\y, 0, \\n, 0\n H3RA \\y, 0, \\n, 1\n"
                "H3MM %1, \\x, \\x+20\n H3RU \\y, 0, \\n\n"
                "H3MM %2, \\x, \\x+24\n H3RU \\y, 1, \\n\n"
                "H3MM %3, \\x, \\x+28\n H3RU \\y, 2, \\n\n"
                "s_waitcnt lgkmcnt(9)\n"
                "H3MM %1, \\x+4, \\x+16\n H3RU \\y, 3, \\n\n"
                "H3MM %2, \\x+4, \\x+20\n H3RA \\y, 1, \\n, 0\n H3RA \\y, 1, \\n, 1\n"
                "H3MM %3, \\x+4, \\x+24\n H3RA \\y, 2, \\n, 0\n"
                "s_waitcnt lgkmcnt(9)\n"
                "H3MM %2, \\x+8, \\x+16\n H3RA \\y, 2, \\n, 1\n"
                "H3MM %3, \\x+8, \\x+20\n H3RA \\y, 3, \\n, 0\n"
                "H3MM %3, \\x+12, \\x+16\n H3RA \\y, 3, \\n, 1\n"
                ".endm\n"
                "H3RA 64, 0, 0, 0\n H3RA 64, 0, 0, 1\n H3RU 64, 0, 0\n H3RU 64, 1, 0\n H3RU 64, 2, 0\n H3RU 64, 3, 0\n"
                "H3RA 64, 1, 0, 0\n H3RA 64, 1, 0, 1\n H3RA 64, 2, 0, 0\n H3RA 64, 2, 0, 1\n H3RA 64, 3, 0, 0\n H3RA 64, 3, 0, 1\n"
                "s_mov_b32 s60, 4\n"
                ".Lh3_conv:\n"
                "H3STEP 64, 96, 1\n"
                "H3STEP 96, 64, 2\n"
                "H3STEP 64, 96, 3\n"
                "s_sub_u32 s60, s60, 1\n"
                "H3STEP 96, 64, 4\n"
                "v_add_u32 %4, 0x100, %4\n v_add_u32 %5, 0x180, %5\n"
                "s_cmp_lg_u32 s60, 0\n"
                "s_cbranch_scc1 .Lh3_conv\n"
                "s_waitcnt lgkmcnt(0)\n s_nop 7\n"
                ".purgem H3MM\n.purgem H3RA\n.purgem H3RU\n.purgem H3STEP\n"
                : "+v"(acc[0]), "+v"(acc[1]), "+v"(acc[2]), "+v"(acc[3]), "+v"(aA), "+v"(aB)
                :
                : "memory", "scc", "s60", "v64", "v65", "v66", "v67", "v68", "v69", "v70", "v71", "v72", "v73", "v74", "v75", "v76", "v77", "v78", "v79",
                  "v80", "v81", "v82", "v83", "v84", "v85", "v86", "v87", "v88", "v89", "v90", "v91", "v92", "v93", "v94", "v95",
                  "v96", "v97", "v98", "v99", "v100", "v101", "v102", "v103", "v104", "v105", "v106", "v107", "v108", "v109", "v110", "v111",
                  "v112", "v113", "v114", "v115", "v116", "v117", "v118", "v119", "v120", "v121", "v122", "v123", "v124", "v125", "v126", "v127");
#pragma unroll
            for (int c = 0; c < 4; ++c)
                lds_st<u32x2>(L0 + L_YC + ch * YC_CH + (16 * c + l16) * 72 + (16 * it + 4 * g16) * 2, u32x2{pk(acc[c][0], acc[c][1]), pk(acc[c][2], acc[c][3])});
        }
        STAMP(5);
        u32x4 f3[2];
        u32 f3o = wave == 0 ? WS_F3 + (b & 3) * 4096 + lane * 64 : OOB;
        f3[0] = ld4<SC1>(rw, f3o, 0); f3[1] = ld4<SC1>(rw, f3o + 16, 0);
        bar();
        if (wave == 0) {
            const u32 tyc = (u32)__builtin_amdgcn_s_memrealtime();
            u32 sp = 0;
            while (__any(((f3[0].y ^ tag) | (f3[0].w ^ tag) | (f3[1].y ^ tag) | (f3[1].w ^ tag)) != 0) && !spent(sp)) {
                delay<4>();
                asm volatile("" : "+v"(f3o) :: "memory");
                f3[0] = ld4<SC1>(rw, f3o, 0); f3[1] = ld4<SC1>(rw, f3o + 16, 0);
            }
            fence();
            COUNT(27, sp);
            STAMP(23);
            int lag = max(max((int)(f3[0].x - tur), (int)(f3[0].z - tur)), max((int)(f3[1].x - tur), (int)(f3[1].z - tur)));
            lag = max(lag, __builtin_amdgcn_update_dpp(0, lag, 0xB1, 0xf, 0xf, true));
            lag = max(lag, __builtin_amdgcn_update_dpp(0, lag, 0x4E, 0xf, 0xf, true));
            lag = max(lag, __builtin_amdgcn_update_dpp(0, lag, 0x124, 0xf, 0xf, true));
            lag = max(lag, __builtin_amdgcn_update_dpp(0, lag, 0x128, 0xf, 0xf, true));
            lag = max(max(__builtin_amdgcn_readlane(lag, 0), __builtin_amdgcn_readlane(lag, 16)), max(__builtin_amdgcn_readlane(lag, 32), __builtin_amdgcn_readlane(lag, 48)));
            lag = min(lag, 1000) + Y_WAIT;
            COUNT(33, lag);
            while ((int)((u32)__builtin_amdgcn_s_memrealtime() - tyc) < lag) __builtin_amdgcn_s_sleep(1);
        }
        STAMP(25);
        bar();
        delay<2>();
        if (QW == 2) {
            const u32 so = WS_WQ + (rank * 96 + wave * 8) * 1024, la = L0 + L_W + wave * 8192;
            asm volatile("s_nop 4\n\ts_mov_b32 m0, %0\n\ts_nop 0\n\t"
                         "buffer_load_dwordx4 %2, %3, %4 offen sc1 lds\n\tbuffer_load_dwordx4 %2, %3, %4 offen offset:1024 sc1 lds\n\t"
                         "buffer_load_dwordx4 %2, %3, %4 offen offset:2048 sc1 lds\n\tbuffer_load_dwordx4 %2, %3, %4 offen offset:3072 sc1 lds\n\t"
                         "s_mov_b32 m0, %1\n\ts_nop 0\n\t"
                         "buffer_load_dwordx4 %2, %3, %5 offen sc1 lds\n\tbuffer_load_dwordx4 %2, %3, %5 offen offset:1024 sc1 lds\n\t"
                         "buffer_load_dwordx4 %2, %3, %5 offen offset:2048 sc1 lds\n\tbuffer_load_dwordx4 %2, %3, %5 offen offset:3072 sc1 lds"
                         ::"s"(la), "s"(la + 4096), "v"(lane * 16), "s"(rw), "s"(so), "s"(so + 4096) : "memory");
        }
        clears(4);
        drain();
        STAMP(34);
        const u32 T = grp * 4 + wave;
        const u32 aW = L0 + L_W + lane * 16;
        u32 aW1 = aW + 33 * 1024, aW2 = aW + 66 * 1024;
        asm volatile("" : "+v"(aW1), "+v"(aW2));
#pragma unroll 1
        for (int layer = 0; layer < 2; ++layer) {
            bar();
            STAMP(6 + 10 * layer);
            delay<DLY_X>();
            const u32 xb = WS_XF + layer * MB2 + T * 32768;
            u32 xo = lane * 16;
            u32x4 X[32];
            f32x16 aK, aV;
            u32 tries = 0;
            while (true) {
                asm volatile("" : "+v"(xo) :: "memory");
#pragma unroll
                for (int i = 0; i < XWIN; ++i) X[i] = ld4<SC1>(rw, xo + (i & 3) * 1024, xb + (i >> 2) * 4096);
                aK = f32x16{}; aV = f32x16{};
                u32x4 wk[3], wv[3];
#pragma unroll
                for (int i = 0; i < 2; ++i) { wk[i] = lds_ld<u32x4>(aW1 + i * 1024); wv[i] = lds_ld<u32x4>(aW2 + i * 1024); }
                SB();
#pragma unroll
                for (int i = 0; i < 32; ++i) {
                    asm volatile("" ::"v"(wv[i % 3]));
                    SB();
                    aK = mfma32(wk[i % 3], X[i], aK);
                    if (i == 0) STAMP(24 + 16 * (1 - layer)); if (i == 20) STAMP(26 + 16 * (1 - layer));
                    SB();
                    if (i < 30) wk[(i + 2) % 3] = lds_ld<u32x4>(aW1 + (i + 2) * 1024);
                    if (i + XWIN < 32) X[(i + XWIN) & 31] = ld4<SC1>(rw, xo + ((i + XWIN) & 3) * 1024, xb + ((i + XWIN) >> 2) * 4096);
                    SB();
                    aV = mfma32(wv[i % 3], X[i], aV);
                    SB();
                    if (i == BS_AT && tries == 0) asm volatile("s_barrier" ::: "memory");
                    if (i < 30) wv[(i + 2) % 3] = lds_ld<u32x4>(aW2 + (i + 2) * 1024);
                    SB();
                }
                if (!__any(aK[0] != aK[0])) break;
                u32 m = 0;
#pragma unroll
                for (int i = 0; i < 32; ++i) m = max4(m, X[i]);
                if (!__any(m == SENT) || ++tries > 4096) break;
            }
            COUNT(28 + layer, tries);
            STAMP(7 + 10 * layer);
            const u32x2 ln = lds_ld<u32x2>(L0 + L_LN + (wave * 32 + rho) * 8);
            const u32x4 xm = {ln.x, ln.x, ln.x, ln.x};
            aK = mfma32(lds_ld<u32x4>(aW1 + 32 * 1024), xm, aK);
            aV = mfma32(lds_ld<u32x4>(aW2 + 32 * 1024), xm, aV);
            const float rs = fasu(ln.y);
            const u32 aI = L0 + L_IMG + (wave * 32 + rho) * IMG_RS;
            u32 phi[16];
            hedgehog(aK, rs, phi);
#pragma unroll
            for (int s = 0; s < 2; ++s)
                lds_st<u32x4>(aI + 128 + hh * 32 + s * 16, u32x4{pk(aV[8 * s] * rs, aV[8 * s + 1] * rs), pk(aV[8 * s + 2] * rs, aV[8 * s + 3] * rs),
                                                                 pk(aV[8 * s + 4] * rs, aV[8 * s + 5] * rs), pk(aV[8 * s + 6] * rs, aV[8 * s + 7] * rs)});
#pragma unroll
            for (int s = 0; s < 4; ++s) lds_st<u32x4>(aI + hh * 64 + s * 16, u32x4{phi[4 * s], phi[4 * s + 1], phi[4 * s + 2], phi[4 * s + 3]});
            STAMP(8 + 10 * layer);
            bar();
            f32x16 aQ = {};
            {
                u32x4 wq[4];
#pragma unroll
                for (int i = 0; i < 3; ++i) wq[i] = lds_ld<u32x4>(aW + i * 1024);
                SB();
#pragma unroll
                for (int i = 0; i < 33; ++i) {
                    if (i + 3 < 33) wq[(i + 3) & 3] = lds_ld<u32x4>(aW + (i + 3) * 1024);
                    aQ = mfma32(wq[i & 3], i < 32 ? X[i & 31] : xm, aQ);
                    SB();
                }
            }
            if (layer == 0) {
#pragma unroll
                for (int s = 0; s < 2; ++s) {
                    st4<SC1>(sent4, rw, xoff + s * 1024, WS_XF + MB2);
                    st4<SC1>(sent4, rw, ooff + s * 1024, WS_OF + MB2);
                    st4<SC1>(sent4, rw, doff, WS_DS + (s + 1) * DS_BUF);
                }
            }
            SB();
            hedgehog(aQ, rs, phi);
#pragma unroll
            for (int i = 0; i < 16; ++i) asm volatile("" : "+v"(phi[i]));
            drain();
            u32 aN = ((rho >> 2) & 1) == hh ? L0 + L_KVT + (hh * 16 + (rho & 3) + 4 * (rho >> 3)) * KVT_RS : L0 + L_ZERO;
            u32 aZ = (l16 >> 2) == g16 ? L0 + L_Z + (g16 >> 1) * 64 : L0 + L_ZERO;
            u32 oo = lane * 16 + WS_OF + ((layer * 64 + T) * 32 + 2 * rank) * 1024;
            asm volatile("" : "+v"(aN), "+v"(aZ), "+v"(oo));
            STAMP(9 + 10 * layer);
            bar();
            STAMP(10 + 10 * layer);
            {
                f32x16 num = {}; f32x4 den = {};
#pragma unroll
                for (int s = 0; s < 4; ++s) den = mfma16(lds_ld<u32x4>(aZ + s * 16), u32x4{phi[4 * s], phi[4 * s + 1], phi[4 * s + 2], phi[4 * s + 3]}, den);
#pragma unroll
                for (int s = 0; s < 4; ++s) num = mfma32(lds_ld<u32x4>(aN + s * 16), u32x4{phi[4 * s], phi[4 * s + 1], phi[4 * s + 2], phi[4 * s + 3]}, num);
                const float r = __builtin_amdgcn_rcpf(den[0] + 1e-6f);
#pragma unroll
                for (int s = 0; s < 2; ++s)
                    st4<SC1>(nos4(u32x4{pk(num[8 * s] * r, num[8 * s + 1] * r), pk(num[8 * s + 2] * r, num[8 * s + 3] * r),
                                        pk(num[8 * s + 4] * r, num[8 * s + 5] * r), pk(num[8 * s + 6] * r, num[8 * s + 7] * r)}), rw, oo + s * 1024, 0);
            }
            STAMP(11 + 10 * layer);
            bar();
            delay<DLY_O>();
            {
                u32x4 o[4];
                const u32 sp = gather4<4, 2, 1024>(o, rw, lane * 16, WS_OF + ((layer * 64 + grp * 4 + (rank >> 2)) * 32 + wave * 4) * 1024);
                COUNT(30 + layer, sp);
#pragma unroll
                for (int i = 0; i < 4; ++i) lds_st<u32x4>(L0 + L_O + (wave * 4 + i) * 1024 + lane * 16, o[i]);
            }
            STAMP(12 + 10 * layer);
            bar();
        }
        DUMP();
        asm volatile(".Lh3_end0:");
        __builtin_amdgcn_endpgm();
    } else {
        __builtin_amdgcn_s_setprio(3);
        const u32 v = wave - 4, dt = tid - 256;
        const u32 T = Td;
        const u32 tok = T * 32 + 4 * (rho & 7) + (rho >> 3);
        const u32 roff = (tok * D + tile * 32 + hh * 8) * 2;
        u32x4 xr[2];
#pragma unroll
        for (int s = 0; s < 2; ++s) xr[s] = ld4<0>(rq, roff + s * 32, 0);
        const u32 junk = ld1<0>(mkrs((const void*)pc0, wave == 7 ? codelen : 0), slot * 1024 + (lane & 15) * 64, 0);
        const u32 dD = ld1<0>(mkrs(gD, D * 2), pair * 4, 0);
        if (dt < 180) { const u32 c = dt >= 90; lds_st<u32x4>(L0 + L_UR + c * UR_CH + 64 * 96 + (dt - c * 90) * 16, u32x4{0, 0, 0, 0}); }
        if (dt == 0) lds_st<u32>(L0 + L_CNT, 0);
        clears(0);
        phase0();
        STAMP(37);
        drain();
        st1<SC1>(tag, rw, ofl, 0);
        clears(2);
        STAMP(36);
        asm volatile("" ::"v"(junk));
        u32x4 R[32];
        u32 cp[6];
        f32x2 xf[8], b1[8], b2[8];
        {
            u32x4 kv[2]; u32 au[2], ay[2];
#pragma unroll
            for (int i = 0; i < 2; ++i) {
                const u32 t0 = 4 * (v * 128 + i * 64 + lane), i0 = 2044 - t0;
                au[i] = L0 + L_UR + (i0 >> 5) * 96 + (i0 & 31) * 2; ay[i] = L0 + L_YC + (t0 >> 5) * 72 + (t0 & 31) * 2;
            }
            f32x2 d1 = up2(dD) * 65537.f, d2 = up2(dD) * -65536.f;
            u32 selLo = ONE_LO, selHi = ONE_HI, oy = WS_YP + qoff;
            asm volatile("" : "+s"(selLo), "+s"(selHi), "+v"(au[0]), "+v"(au[1]), "+v"(ay[0]), "+v"(ay[1]), "+v"(oy), "+v"(d1), "+v"(d2));
            delay<DLY_F1>();
            { const u32 sp = wait_flags<1, 2>(rw, (pair * 4 + v) * 128 + c8 * 4, WS_F1, tag); COUNT(26, sp); }
            STAMP(4);
#pragma unroll
            for (int i = 0; i < 2; ++i) kv[i] = ld4<SC1>(rw, qoff + i * 1024, WS_KVP);
            const u32 fo = WS_FW + ((rank * 16 + grp) * 4 + v) * 128 + (l16 < 12 ? l16 : 0) * 4;
            const u32 fw = ld1<SC1>(rw, fo, 0);
            SB();
#pragma unroll
            for (int i = 0; i < 2; ++i) {
                lds_st<u32x2>(au[i], u32x2{__builtin_amdgcn_perm(kv[i].z, kv[i].w, 0x05040100u), __builtin_amdgcn_perm(kv[i].x, kv[i].y, 0x05040100u)});
                lds_st<u32x2>(au[i] + UR_CH, u32x2{__builtin_amdgcn_perm(kv[i].z, kv[i].w, 0x07060302u), __builtin_amdgcn_perm(kv[i].x, kv[i].y, 0x07060302u)});
            }
            f32x2 tD[8];
#pragma unroll
            for (int i = 0; i < 8; ++i) { const f32x2 k2 = up2(kv[i >> 2][i & 3]); tD[i] = pfma(k2, d2, k2 * d1); asm volatile("" : "+v"(tD[i])); }
            STAMP(5);
            bar();
            const u32 tur = (u32)__builtin_amdgcn_s_memrealtime();
            constexpr bool LATE3 = CY == 3 || CX == 3 || CO == 3 || CZ == 3;
            if (!LATE3) st2<SC1>(u32x2{tur, tag}, rw, dt < 4 ? WS_F3 + lane * 4096 + (b >> 2) * 64 + (b & 3) * 8 : OOB, 0);
            clears(3);
            COUNT(42, 1);
            if (!__all(fw == tag)) { wait_flags<1, 2>(rw, fo, 0, tag); COUNT(42, 0); }
            fence();
            u32x2 st0 = __builtin_amdgcn_raw_buffer_load_b64(rw, tok * 8, WS_ST0, SC1);
#pragma unroll
            for (int i = 0; i < 6; ++i) cp[i] = ld1<SC1>(rw, WS_CS + (rank * 64 + v * 4 + hh * 2) * 128 + rho * 4 + (i & 1) * 128, (i >> 1) * 2048);
#pragma unroll
            for (int i = 0; i < 8; ++i) R[8 + i] = ld4<SC1>(rw, lane * 16 + (i & 3) * 1024, WS_WQ + ((rank * 3 + 1) * 32 + v * 8 + (i & 4)) * 1024);
            if (LATE3) {
                asm volatile("s_waitcnt vmcnt(15)" ::: "memory");
                u32 old = 0;
                if (lane == 0) old = __hip_atomic_fetch_add((LAS u32*)(size_t)(L0 + L_CNT), 1u, __ATOMIC_RELAXED, __HIP_MEMORY_SCOPE_WORKGROUP);
                st2<SC1>(u32x2{tur, tag}, rw, __builtin_amdgcn_readfirstlane(old) == 3 && lane < 4 ? WS_F3 + lane * 4096 + (b >> 2) * 64 + (b & 3) * 8 : OOB, 0);
            }
            STAMP(34);
#pragma unroll
            for (int i = 0; i < 8; ++i) R[16 + i] = ld4<SC1>(rw, lane * 16 + (i & 3) * 1024, WS_WQ + ((rank * 3 + 2) * 32 + v * 8 + (i & 4)) * 1024);
#pragma unroll
            for (int i = 0; i < 8; ++i) if (QW == 1) R[i] = ld4<SC1>(rw, lane * 16 + (i & 3) * 1024, WS_WQ + ((rank * 3) * 32 + v * 8 + (i & 4)) * 1024);
            STAMP(14);
            bar();
            STAMP(15);
            u32x2 ya[2], yb[2];
#pragma unroll
            for (int i = 0; i < 2; ++i) { ya[i] = lds_ld<u32x2>(ay[i]); yb[i] = lds_ld<u32x2>(ay[i] + YC_CH); }
#pragma unroll
            for (int i = 0; i < 2; ++i) {
                u32x4 y;
#pragma unroll
                for (int e = 0; e < 4; ++e) {
                    const u32 sel = (e & 1) ? selHi : selLo;
                    y[e] = nos(pk(dot2(ya[i][e >> 1], sel, tD[4 * i + e].x), dot2(yb[i][e >> 1], sel, tD[4 * i + e].y)));
                }
                st4<SC1>(y, rw, oy + i * 1024, 0);
            }
            SB();
            STAMP(23);
            if (__any(st0.y != tag)) {
                u32 sp = 0;
                do { fence(); st0 = __builtin_amdgcn_raw_buffer_load_b64(rw, tok * 8, WS_ST0, SC1); } while (__any(st0.y != tag) && !spent(sp));
            }
            const float rs = __builtin_amdgcn_rcpf(hi(st0.x)) * 1.0000003f, mean = lo(st0.x);
#pragma unroll
            for (int i = 0; i < 8; ++i) {
                xf[i] = up2(xr[i >> 2][i & 3]);
                const f32x2 n = rb2(rb2(xf[i] - mean) * rs);
                b1[i] = n * 65537.f; b2[i] = n * -65536.f;
                asm volatile("" : "+v"(xf[i]), "+v"(b1[i]), "+v"(b2[i]));
            }
        }
        STAMP(25);
        bar();
        u32x4 yt[2];
        const u32 poff = ((tile * 16 + (lane >> 3)) * 2048 + T * 32 + 4 * (lane & 7)) * 4;
#pragma unroll
        for (int s = 0; s < 2; ++s) yt[s] = ld4<SC1>(rw, poff, WS_YP + s * 65536);
        SB();
#pragma unroll
        for (int i = QW == 1 ? 0 : 8; i < 24; ++i) lds_st<u32x4>(L0 + L_W + ((i >> 3) * 33 + v * 8 + (i & 7)) * 1024 + lane * 16, R[i]);
        SB();
        { u32 sp = 0; if (__any(max4(max4(0, yt[0]), yt[1]) == SENT)) sp = 1 + gather4<2, 2, 65536>(yt, rw, poff, WS_YP); COUNT(41, sp); }
        STAMP(13);
#pragma unroll
        for (int s = 0; s < 2; ++s) yt[s] = t1(yt[s]);
#pragma unroll
        for (int i = 0; i < 8; ++i) { const f32x2 y = up2(yt[i >> 2][i & 3]); xr[i >> 2][i & 3] = nos(pk2(xf[i] + pfma(y, b2[i], y * b1[i]))); }
#pragma unroll
        for (int s = 0; s < 2; ++s) st4<SC1>(xr[s], rw, xoff + s * 1024, WS_XF);
        SB();
        STAMP(3);
        bar();
        stats_publish(xr, rw, lane, WS_DS + (T * 16 + tile) * 256);
#pragma unroll
        for (int p = 0; p < 3; ++p) {
            const float cs = halfsum(fasu(cp[2 * p]) + fasu(cp[2 * p + 1]));
            const float c0 = rb(cs), c1 = rb(cs - c0), c2 = cs - c0 - c1;
            lds_st<u32x2>(L0 + L_W + (p * 33 + 32) * 1024 + ((v >> 1) * 32 + rho) * 16 + (v & 1) * 8, u32x2{pk(c0, c1), pk(c2, 0.f)});
        }
        if (dt < 4) lds_st<u32x4>(L0 + L_ZERO + lane * 16, u32x4{0, 0, 0, 0});
        u32 aO = L0 + L_O + lane * 16;
        asm volatile("" : "+v"(aO));
        auto ln_to_lds = [&](u32 buf) __attribute__((always_inline)) {
            float mean, rs;
            const u32 sp = stats_gather(rw, lane, WS_DS + buf * DS_BUF + (grp * 4 + v) * 4096, mean, rs);
            COUNT(21, sp);
            if (lane < 32) lds_st<u32x2>(L0 + L_LN + (v * 32 + lane) * 8, u32x2{pk(-mean, -mean), uasf(rs)});
        };
        delay<(DLY_SC > 5 ? DLY_SC - 5 : 0)>();
        ln_to_lds(0);
        STAMP(7);
        bar();
#pragma unroll
        for (int i = 0; i < 32; ++i) R[i] = ld4<SC1>(rw, lane * 16 + (i & 3) * 1024, WS_WO + (tile * 32 + (i & ~3)) * 1024);
        STAMP(35);
#pragma unroll 1
        for (int layer = 0; layer < 2; ++layer) {
            bar();
            STAMP(6 + 10 * layer);
            const u32 head = v >> 1, fh = v & 1;
            const u32 spb = WS_SP + (layer * 16 + rank) * 16 * SP_UNIT;
            {
                const u32 a = L0 + L_IMG + (4 * g16 + (l16 >> 2)) * IMG_RS + (l16 & 3) * 8;
                f32x4 kvp = {}, zp = {};
                const u32x4 ones = {ONE2, ONE2, ONE2, ONE2};
#pragma unroll
                for (int ks = 0; ks < 4; ++ks) {
                    const u32 r0 = a + ks * 32 * IMG_RS, r1 = r0 + 16 * IMG_RS;
                    const u32x2 p0 = lds_tr(r0 + head * 64 + fh * 32), p1 = lds_tr(r1 + head * 64 + fh * 32);
                    const u32x2 v0 = lds_tr(r0 + 128 + head * 32), v1 = lds_tr(r1 + 128 + head * 32);
                    const u32x4 pa = {p0.x, p0.y, p1.x, p1.y};
                    kvp = mfma16(pa, u32x4{v0.x, v0.y, v1.x, v1.y}, kvp);
                    zp = mfma16(pa, ones, zp);
                }
                st2<SC1>(u32x2{nos(pk(kvp[0], kvp[1])), nos(pk(kvp[2], kvp[3]))}, rw, v * 512 + l16 * 32 + g16 * 8, spb + grp * SP_UNIT);
                st4<SC1>(nos4(__builtin_bit_cast(u32x4, zp)), rw, l16 == 0 ? 2048 + v * 64 + g16 * 16 : OOB, spb + grp * SP_UNIT);
            }
            STAMP(layer ? 39 : 38);
            delay<DLY_ST>();
            {
                u32x4 pt[8]; u32 zt[4]; u32 sp = 0;
                while (true) {
                    fence();
#pragma unroll
                    for (int i = 0; i < 8; ++i) pt[i] = ld4<SC1>(rw, (g16 >> 1) * SP_UNIT + v * 512 + l16 * 32 + (g16 & 1) * 16, spb + 2 * i * SP_UNIT);
#pragma unroll
                    for (int i = 0; i < 4; ++i) zt[i] = ld1<SC1>(rw, g16 * SP_UNIT + 2048 + v * 64 + l16 * 4, spb + 4 * i * SP_UNIT);
                    u32 m = 0;
#pragma unroll
                    for (int i = 0; i < 8; ++i) m = max4(m, pt[i]);
                    m = max(max(m, max(zt[0], zt[1])), max(zt[2], zt[3]));
                    if (!__any(m == SENT) || spent(sp)) break;
                    delay<2>();
                }
                COUNT(28 + layer, sp);
                const u32 e = l16 & 7, one = (l16 >> 3) == (g16 & 1) ? ((e & 1) ? ONE_HI : ONE_LO) : 0u;
                const u32x4 idm = {(e >> 1) == 0 ? one : 0u, (e >> 1) == 1 ? one : 0u, (e >> 1) == 2 ? one : 0u, (e >> 1) == 3 ? one : 0u};
                f32x4 t = {};
#pragma unroll
                for (int i = 0; i < 8; ++i) t = mfma16(idm, pt[i], t);
                const float z = halfsum(rowpairsum((fasu(zt[0]) + fasu(zt[1])) + (fasu(zt[2]) + fasu(zt[3]))));
                lds_st<u32x2>(L0 + L_KVT + (head * 16 + l16) * KVT_RS + fh * 32 + g16 * 8, u32x2{pk(t[0], t[1]), pk(t[2], t[3])});
                if (lane < 16) lds_st<u16>(L0 + L_Z + head * 64 + fh * 32 + lane * 2, (u16)pk(z, z));
            }
            STAMP(8 + 10 * layer);
            bar();
            bar();
            delay<DLY_O>();
            {
                u32x4 o[4];
                const u32 sp = gather4<4, 2, 1024>(o, rw, lane * 16, WS_OF + ((layer * 64 + T) * 32 + wave * 4) * 1024);
                COUNT(30 + layer, sp);
#pragma unroll
                for (int i = 0; i < 4; ++i) lds_st<u32x4>(L0 + L_O + (wave * 4 + i) * 1024 + lane * 16, o[i]);
            }
            bar();
            STAMP(9 + 10 * layer);
            f32x16 acc;
#pragma unroll
            for (int i = 0; i < 8; ++i) { acc[2 * i] = lo(xr[i >> 2][i & 3]); acc[2 * i + 1] = hi(xr[i >> 2][i & 3]); }
            {
                u32x4 of[4];
#pragma unroll
                for (int i = 0; i < 3; ++i) of[i] = lds_ld<u32x4>(aO + i * 1024);
                SB();
#pragma unroll
                for (int i = 0; i < 32; ++i) {
                    if (i + 3 < 32) of[(i + 3) & 3] = lds_ld<u32x4>(aO + (i + 3) * 1024);
                    acc = mfma32(R[i], of[i & 3], acc);
                    SB();
                }
            }
#pragma unroll
            for (int i = 0; i < 8; ++i) xr[i >> 2][i & 3] = pk(acc[2 * i], acc[2 * i + 1]);
            STAMP(10 + 10 * layer);
            if (layer == 1) {
                const rsrc_t ro = mkrs(gout, S * D * 2);
                st4<0>(xr[0], ro, roff, 0);
                st4<0>(xr[1], ro, roff + 32, 0);
                STAMP(22);
                break;
            }
            stats_publish(xr, rw, lane, WS_DS + DS_BUF + (T * 16 + tile) * 256);
            asm volatile("" : "+v"(yt[0]), "+v"(yt[1]));
#pragma unroll
            for (int i = 0; i < 8; ++i) {
                xf[i] = up2(xr[i >> 2][i & 3]);
                const f32x2 y = up2(yt[i >> 2][i & 3]);
                b1[i] = y * 65537.f; b2[i] = y * -65536.f;
                asm volatile("" : "+v"(xf[i]), "+v"(b1[i]), "+v"(b2[i]));
            }
            delay<DLY_DS>();
            float mean, rs;
            { const u32 sp = stats_gather(rw, lane, WS_DS + DS_BUF + T * 4096, mean, rs); COUNT(24, sp); }
            STAMP(11);
#pragma unroll
            for (int i = 0; i < 8; ++i) {
                const f32x2 n = rb2(rb2(xf[i] - mean) * rs);
                xr[i >> 2][i & 3] = nos(pk2(xf[i] + pfma(n, b2[i], n * b1[i])));
            }
#pragma unroll
            for (int s = 0; s < 2; ++s) st4<SC1>(xr[s], rw, xoff + s * 1024, WS_XF + MB2);
            SB();
            STAMP(12);
            bar();
            stats_publish(xr, rw, lane, WS_DS + 2 * DS_BUF + (T * 16 + tile) * 256);
            delay<DLY_SC>();
            ln_to_lds(2);
            STAMP(17);
            bar();
        }
        if (b == 0 && tid == 256) *(u32*)(ws + WS_E) = tag;
        DUMP();
        asm volatile(".Lh3_end1:");
        __builtin_amdgcn_endpgm();
    }
}

extern "C" void kernel_launch(void* const* d_in, const int* in_sizes, int n_in, void* d_out, int out_size, void* d_ws, size_t ws_size, hipStream_t stream) {
    (void)hipFuncSetAttribute((const void*)h3_hedgehog_kernel, hipFuncAttributeMaxDynamicSharedMemorySize, LDS_BYTES);
    h3_hedgehog_kernel<<<256, 512, LDS_BYTES, stream>>>((const u16*)d_in[0], (const u16*)d_in[1], (const u16*)d_in[2], (const u16*)d_in[3], (const u16*)d_in[4],
                                                        (const u16*)d_in[5], (const u16*)d_in[7], (const u16*)d_in[8], (const u16*)d_in[9], (u16*)d_out, (char*)d_ws);
}
```

```cpp
#include <hip/hip_runtime.h>
#include <stdint.h>

typedef unsigned u32;
typedef unsigned short u16;
typedef unsigned long long u64;
typedef u32 u32x4 __attribute__((ext_vector_type(4)));
typedef u32 u32x2 __attribute__((ext_vector_type(2)));
typedef float f32x2 __attribute__((ext_vector_type(2)));
typedef float f32x4 __attribute__((ext_vector_type(4)));
typedef float f32x16 __attribute__((ext_vector_type(16)));
typedef __bf16 bf16x2 __attribute__((ext_vector_type(2)));
typedef __bf16 bf16x8 __attribute__((ext_vector_type(8)));
typedef short s16x4 __attribute__((ext_vector_type(4)));
typedef __amdgpu_buffer_rsrc_t rsrc_t;
#define LAS __attribute__((address_space(3)))
#define DI __device__ __forceinline__

#define DLY_F1 6
#define DLY_SC 9
#define XWIN 12
#define XGAP 1
#define MAP48 1
#define H1SAME 0
#define Y_WAIT 45
#define ST_WAIT 34
#define DLY_O 14
#define DLY_DS 10
#define DLY_X 14
#define CX 1
#define CL 2
#define WO_W2 0
#define SC_WAIT 40
#define O_DMA 0
#define XFIX 1
#define CODE_PF 2
#define XFIX_N 16
#define PF_ORDER 0
#define BS_AT 20
#define DLY_P2 5
#define TS_PACE 0

constexpr int S = 2048, D = 512;
constexpr u32 MB2 = 2u << 20;
constexpr u32 WS_WQ = 0, WS_WO = 0x180000, WS_KVP = MB2, WS_DS = 2 * MB2, DS_BUF = 64 * 16 * 256, WS_YP = 3 * MB2, WS_XF = 4 * MB2,
              WS_OF = 6 * MB2, WS_SP = 8 * MB2, SP_UNIT = 2560, SP_WAVE = 640,
              WS_STAMP = 9 * MB2, WS_CTRL = WS_STAMP + 0x100000, WS_E = WS_CTRL, WS_F1 = WS_CTRL + 4096, WS_FW = WS_F1 + 2048 * 128, WS_F3 = WS_FW + 1024 * 128,
              WS_ST0 = WS_F3 + 4 * 4096, WS_CS = WS_ST0 + 1024 * 128, WS_TS = WS_CS + 1024 * 128, WS_BYTES = 10 * MB2;
static_assert(WS_TS + 512 * 128 <= WS_BYTES, "ws");
constexpr u32 OOB = 0x80000000u;
constexpr u32 L_W = 0, L_R2 = 3 * 33 * 1024;
constexpr u32 L_HC = L_R2, HC_COPY = 4160, HC_CH = 4 * HC_COPY, L_UR = L_HC + 2 * HC_CH, UR_CH = 79 * 96, L_YC = L_UR + 2 * UR_CH, YC_CH = 64 * 72;
constexpr u32 L_PK = 0  , L_PV = 72 * 128, L_PH = L_PV + 64 * 128, L_SA = L_PH + 1024;
constexpr u32 L_IMG = L_R2, IMG_RS = 224, L_O = L_R2, L_KVT = L_R2 + 32768, KVT_RS = 80, L_Z = L_KVT + 32 * KVT_RS, L_ZERO = L_Z + 128, L_LM = L_ZERO + 64, L_LR = L_LM + 128 * 16, L_OK = L_LR + 128 * 8, L_RL = L_OK + 16;
constexpr u32 LDS_BYTES = 163840;
static_assert(L_YC + 2 * YC_CH <= LDS_BYTES && L_ZERO + 64 <= LDS_BYTES, "lds");
constexpr int SC1 = 16;
constexpr u32 SENT = 0xFFFFFFFFu;
constexpr u32 SPIN_CAP = 1u << 15;

DI u32 pk(float a, float b) { return __builtin_bit_cast(u32, __builtin_convertvector(f32x2{a, b}, bf16x2)); }
DI float lo(u32 x) { return __builtin_bit_cast(float, x << 16); }
DI float hi(u32 x) { return __builtin_bit_cast(float, x & 0xFFFF0000u); }
DI float rb(float x) { return lo(pk(x, x)); }
DI float dot2(u32 a, u32 b, float c) { return __builtin_amdgcn_fdot2_f32_bf16(__builtin_bit_cast(bf16x2, a), __builtin_bit_cast(bf16x2, b), c, false); }
DI f32x2 pfma(f32x2 a, f32x2 b, f32x2 c) { return __builtin_elementwise_fma(a, b, c); }
DI f32x2 rb2(f32x2 x) { return pfma(x, f32x2{-65536.f, -65536.f}, x * 65537.f); }
DI f32x2 up2(u32 x) { return f32x2{lo(x), hi(x)}; }
DI u32 pk2(f32x2 x) { return pk(x.x, x.y); }
DI f32x16 mfma32(u32x4 a, u32x4 b, f32x16 c) { return __builtin_amdgcn_mfma_f32_32x32x16_bf16(__builtin_bit_cast(bf16x8, a), __builtin_bit_cast(bf16x8, b), c, 0, 0, 0); }
DI f32x4 mfma16(u32x4 a, u32x4 b, f32x4 c) { return __builtin_amdgcn_mfma_f32_16x16x32_bf16(__builtin_bit_cast(bf16x8, a), __builtin_bit_cast(bf16x8, b), c, 0, 0, 0); }
DI rsrc_t mkrs(const void* p, u32 bytes) { return __builtin_amdgcn_make_buffer_rsrc((void*)p, 0, bytes, 0x00020000); }
template <int AUX> DI u32x4 ld4(rsrc_t r, u32 v, u32 s) { return __builtin_amdgcn_raw_buffer_load_b128(r, v, s, AUX); }
template <int AUX> DI u32 ld1(rsrc_t r, u32 v, u32 s) { return __builtin_amdgcn_raw_buffer_load_b32(r, v, s, AUX); }
template <int AUX> DI void st4(u32x4 x, rsrc_t r, u32 v, u32 s) { __builtin_amdgcn_raw_buffer_store_b128(x, r, v + s, 0, AUX); }
template <int AUX> DI void st2(u32x2 x, rsrc_t r, u32 v, u32 s) { __builtin_amdgcn_raw_buffer_store_b64(x, r, v, s, AUX); }
template <int AUX> DI void st1(u32 x, rsrc_t r, u32 v, u32 s) { __builtin_amdgcn_raw_buffer_store_b32(x, r, v, s, AUX); }
DI void dma(rsrc_t r, u32 v, u32 ldsaddr) {
    asm volatile("s_mov_b32 m0, %0\n\ts_nop 0\n\tbuffer_load_dwordx4 %1, %2, 0 offen lds" ::"s"(ldsaddr), "v"(v), "s"(r) : "memory");
}
template <typename T> DI T lds_ld(u32 a) { return *(LAS T*)(size_t)a; }
template <typename T> DI void lds_st(u32 a, T v) { *(LAS T*)(size_t)a = v; }
DI u32x2 lds_tr(u32 a) { return __builtin_bit_cast(u32x2, __builtin_amdgcn_ds_read_tr16_b64_v4i16((LAS s16x4*)(size_t)a)); }
DI void bar() { __builtin_amdgcn_sched_barrier(0); asm volatile("s_waitcnt lgkmcnt(0)\n\ts_barrier" ::: "memory"); __builtin_amdgcn_sched_barrier(0); }
DI void drain() { asm volatile("s_waitcnt vmcnt(0)" ::: "memory"); }
DI void fence() { asm volatile("" ::: "memory"); }
DI bool spent(u32& sp) { u32 one; asm volatile("s_mov_b32 %0, 1" : "=s"(one)); sp += one; return sp > SPIN_CAP; }
template <int N> DI void delay() { if (N > 0) __builtin_amdgcn_s_sleep(N); }
DI u32 nos(u32 x) { return x < 0xFFFFFFFEu ? x : 0xFFFFFFFEu; }
DI u32x4 nos4(u32x4 x) { return u32x4{nos(x.x), nos(x.y), nos(x.z), nos(x.w)}; }
DI u32 max4(u32 m, u32x4 v) { return max(max(max(max(m, v.x), v.y), v.z), v.w); }
DI float fasu(u32 x) { return __builtin_bit_cast(float, x); }
DI u32 uasf(float x) { return __builtin_bit_cast(u32, x); }
DI float halfsum(float x) { auto r = __builtin_amdgcn_permlane32_swap(uasf(x), uasf(x), false, false); return fasu(r[0]) + fasu(r[1]); }
DI float rowpairsum(float x) { auto r = __builtin_amdgcn_permlane16_swap(uasf(x), uasf(x), false, false); return fasu(r[0]) + fasu(r[1]); }
template <int CTRL> DI float dppadd(float x) { return x + fasu(__builtin_amdgcn_update_dpp(0u, uasf(x), CTRL, 0xf, 0xf, true)); }
DI u32x4 t1(u32x4 a) {
    auto r02 = __builtin_amdgcn_permlane16_swap(a.x, a.z, false, false);
    auto r13 = __builtin_amdgcn_permlane16_swap(a.y, a.w, false, false);
    u32 a0 = r02[0], a2 = r02[1], a1 = r13[0], a3 = r13[1];
    u32x4 o;
    o.x = __builtin_amdgcn_update_dpp(a0, a1, 0x128, 0xf, 0xc, false);
    o.y = __builtin_amdgcn_update_dpp(a1, a0, 0x128, 0xf, 0x3, false);
    o.z = __builtin_amdgcn_update_dpp(a2, a3, 0x128, 0xf, 0xc, false);
    o.w = __builtin_amdgcn_update_dpp(a3, a2, 0x128, 0xf, 0x3, false);
    return o;
}
#define SB() __builtin_amdgcn_sched_barrier(0)
DI void lnstat(float sum, float sq, float& m, float& s) {
    float mean = sum * (1.f / D);
    float var = fmaxf(sq * (1.f / D) - mean * mean, 0.f);
    m = rb(mean);
    s = rb(__builtin_amdgcn_sqrtf(rb(rb(var) + 1e-5f)));
}
constexpr u32 ONE_LO = 0x00003F80u, ONE_HI = 0x3F800000u, ONE2 = 0x3F803F80u;

DI float max3(float a, float b, float c) { float r; asm("v_max3_f32 %0, %1, %2, %3" : "=v"(r) : "v"(a), "v"(b), "v"(c)); return r; }
DI float min3(float a, float b, float c) { float r; asm("v_min3_f32 %0, %1, %2, %3" : "=v"(r) : "v"(a), "v"(b), "v"(c)); return r; }
DI float max16(const f32x16& p) { return max3(max3(max3(p[0], p[1], p[2]), max3(p[3], p[4], p[5]), max3(p[6], p[7], p[8])), max3(max3(p[9], p[10], p[11]), max3(p[12], p[13], p[14]), p[15]), p[15]); }
DI float min16(const f32x16& p) { return min3(min3(min3(p[0], p[1], p[2]), min3(p[3], p[4], p[5]), min3(p[6], p[7], p[8])), min3(min3(p[9], p[10], p[11]), min3(p[12], p[13], p[14]), p[15]), p[15]); }
DI void softmax16(const f32x16& p, float c, float a, u32x4& o0, u32x4& o1) {
    f32x2 e[8];
    const f32x2 c2 = {c, c}, a2 = {a, a};
#pragma unroll
    for (int i = 0; i < 8; ++i) {
        const f32x2 t = pfma(f32x2{p[2 * i], p[2 * i + 1]}, c2, a2);
        e[i] = f32x2{__builtin_amdgcn_exp2f(t.x), __builtin_amdgcn_exp2f(t.y)};
    }

    const f32x2 s = ((e[0] + e[1]) + (e[2] + e[3])) + ((e[4] + e[5]) + (e[6] + e[7]));
    const float r = __builtin_amdgcn_rcpf(s.x + s.y);
#pragma unroll
    for (int i = 0; i < 4; ++i) { o0[i] = pk2(e[i] * r); o1[i] = pk2(e[4 + i] * r); }
}
DI u32x4 pk8(const f32x16& a, int s, float r) {
    return u32x4{pk2(f32x2{a[8 * s], a[8 * s + 1]} * r), pk2(f32x2{a[8 * s + 2], a[8 * s + 3]} * r), pk2(f32x2{a[8 * s + 4], a[8 * s + 5]} * r), pk2(f32x2{a[8 * s + 6], a[8 * s + 7]} * r)};
}
DI u32 now() { return (u32)__builtin_amdgcn_s_memrealtime(); }
DI void wait_until(u32 t0, int ticks) { while ((int)(now() - t0) < ticks) __builtin_amdgcn_s_sleep(1); }
DI int rowmax(int x) {
    x = max(x, __builtin_amdgcn_update_dpp(0, x, 0xB1, 0xf, 0xf, true));
    x = max(x, __builtin_amdgcn_update_dpp(0, x, 0x4E, 0xf, 0xf, true));
    x = max(x, __builtin_amdgcn_update_dpp(0, x, 0x124, 0xf, 0xf, true));
    return max(x, __builtin_amdgcn_update_dpp(0, x, 0x128, 0xf, 0xf, true));
}
#define RARE(x) __builtin_expect(!!(x), 0)
DI bool again(bool bad, u32& sp) { if (RARE(bad)) { __builtin_amdgcn_s_sleep(2); return !spent(sp); } return false; }

#define STAMP(i) do { if ((i) == 6) tprobe = (u32)__builtin_amdgcn_s_memrealtime(); } while (0)
#define COUNT(i, v)
#define DUMP() do { if (wave == 4 && (__builtin_amdgcn_s_getreg((3 << 11) | 20) & 0xF) == 5) { const u32 te = (u32)__builtin_amdgcn_s_memrealtime(); while ((u32)__builtin_amdgcn_s_memrealtime() - te < tprobe - tstart) __builtin_amdgcn_s_sleep(1); } } while (0)

DI u32 wait_flags(rsrc_t r, u32 v, u32 tag) {
    u32 sp = 0;
    do { fence(); } while (again(__any(ld1<SC1>(r, v, 0) != tag), sp));
    fence();
    return sp;
}
DI void dma8k(rsrc_t r, u32 v, u32 so, u32 la) {
    asm volatile("s_nop 4\n\ts_mov_b32 m0, %0\n\ts_nop 0\n\t"
                 "buffer_load_dwordx4 %2, %3, %4 offen sc1 lds\n\tbuffer_load_dwordx4 %2, %3, %4 offen offset:1024 sc1 lds\n\t"
                 "buffer_load_dwordx4 %2, %3, %4 offen offset:2048 sc1 lds\n\tbuffer_load_dwordx4 %2, %3, %4 offen offset:3072 sc1 lds\n\t"
                 "s_mov_b32 m0, %1\n\ts_nop 0\n\t"
                 "buffer_load_dwordx4 %2, %3, %5 offen sc1 lds\n\tbuffer_load_dwordx4 %2, %3, %5 offen offset:1024 sc1 lds\n\t"
                 "buffer_load_dwordx4 %2, %3, %5 offen offset:2048 sc1 lds\n\tbuffer_load_dwordx4 %2, %3, %5 offen offset:3072 sc1 lds"
                 ::"s"(la), "s"(la + 4096), "v"(v), "s"(r), "s"(so), "s"(so + 4096) : "memory");
}
template <int IMM> DI void xfix(u32x4& x, u32x4& y, rsrc_t r, u32 v, u32 so, u32& budget) {
    u32 t;
    asm volatile("v_max_u32 %3, %4, %5\n\tv_cmp_eq_u32 vcc, -1, %3\n\ts_cbranch_vccnz 2f\n1:\n"
                 ".subsection 1\n2:\n\ts_cmp_eq_u32 %2, 0\n\ts_cbranch_scc1 1b\n\ts_sub_u32 %2, %2, 1\n\ts_sleep 2\n\t"
                 "buffer_load_dword %3, %6, %7, %8 offen offset:%c9+1024 sc1\n\tbuffer_load_dwordx4 %0, %6, %7, %8 offen offset:%c9 sc1\n\t"
                 "buffer_load_dwordx4 %1, %6, %7, %8 offen offset:%c9+1024 sc1\n\ts_waitcnt vmcnt(0)\n\t"
                 "v_cmp_eq_u32 vcc, -1, %3\n\ts_cbranch_vccnz 2b\n\ts_branch 1b\n.subsection 0"
                 : "+v"(x), "+v"(y), "+s"(budget), "=&v"(t) : "v"(x.x), "v"(y.x), "v"(v), "s"(r), "s"(so), "i"(IMM) : "vcc", "scc", "memory");
}
DI u32 o_gather(rsrc_t r, u32 v, u32 so, u32 la) {
    u32 sp = 0; bool bad;
    do {
        asm volatile("" : "+v"(v) :: "memory");
        u32x4 o[4]; u32 m = 0;
#pragma unroll
        for (int i = 0; i < 4; ++i) o[i] = ld4<SC1>(r, v + i * 1024, so);
        SB();
#pragma unroll
        for (int i = 0; i < 4; ++i) { lds_st<u32x4>(la + i * 1024, o[i]); m = max4(m, o[i]); SB(); }
        bad = __any(m == SENT);
    } while (again(bad, sp));
    return sp;
}
DI void stats_publish(const u32x4 (&x)[2], rsrc_t r, u32 v, u32 off) {
    float s0 = 0.f, s1 = 0.f, q0 = 0.f, q1 = 0.f;
#pragma unroll
    for (int i = 0; i < 4; ++i) { s0 = dot2(x[0][i], ONE2, s0); q0 = dot2(x[0][i], x[0][i], q0); s1 = dot2(x[1][i], ONE2, s1); q1 = dot2(x[1][i], x[1][i], q1); }
    auto p = __builtin_amdgcn_permlane32_swap(uasf(s0 + s1), uasf(q0 + q1), false, false);
    st1<SC1>(nos(uasf(fasu(p[0]) + fasu(p[1]))), r, v, off);
}
DI void stats_issue(u32x4 (&ds)[4], rsrc_t r, u32 v, u32 off) {
#pragma unroll
    for (int i = 0; i < 4; ++i) ds[i] = ld4<SC1>(r, v + i * 1024, off);
}
DI float stats_sum(const u32x4 (&ds)[4]) {
    const f32x4 a = (__builtin_bit_cast(f32x4, ds[0]) + __builtin_bit_cast(f32x4, ds[1])) + (__builtin_bit_cast(f32x4, ds[2]) + __builtin_bit_cast(f32x4, ds[3]));
    auto p0 = __builtin_amdgcn_permlane32_swap(uasf(a[0]), uasf(a[2]), false, false);
    auto p1 = __builtin_amdgcn_permlane32_swap(uasf(a[1]), uasf(a[3]), false, false);
    auto p2 = __builtin_amdgcn_permlane16_swap(uasf(fasu(p0[0]) + fasu(p0[1])), uasf(fasu(p1[0]) + fasu(p1[1])), false, false);
    return fasu(p2[0]) + fasu(p2[1]);
}
DI u32 stats_check(u32x4 (&ds)[4], rsrc_t r, u32 v, u32 off, float& t) {
    u32 sp = 0;
    t = stats_sum(ds);
    if (RARE(__any(t != t))) {
        while (again(__any(max4(max4(max4(max4(0, ds[0]), ds[1]), ds[2]), ds[3]) == SENT), sp)) { asm volatile("" : "+v"(v) :: "memory"); stats_issue(ds, r, v, off); }
        t = stats_sum(ds);
        __builtin_amdgcn_s_waitcnt(0x0F70);
    }
    return sp;
}
DI void stats_finish(float t, float& mean, float& rs) {
    auto p = __builtin_amdgcn_permlane32_swap(uasf(t), uasf(t), false, false);
    float sd; lnstat(fasu(p[0]), fasu(p[1]), mean, sd);
    rs = __builtin_amdgcn_rcpf(sd) * 1.0000003f;
}

typedef u64 u64x8 __attribute__((ext_vector_type(8)));
typedef u64 u64x2 __attribute__((ext_vector_type(2)));
__global__ void __launch_bounds__(512, 2) h3_hedgehog_kernel(const u16*, const u16*, const u16*, const u16*, const u16*, const u16*,
                                                             const u16*, const u16*, const u16*, u16*, char*) {
    u64x8 ka; u64x2 kb; u64 kc;
    asm volatile("s_load_dwordx16 %0, %3, 0x0\n\ts_load_dwordx4 %1, %3, 0x40\n\ts_load_dwordx2 %2, %3, 0x50"
                 : "=&s"(ka), "=&s"(kb), "=&s"(kc) : "s"(__builtin_amdgcn_kernarg_segment_ptr()));
    SB();
    u64 ts_; asm volatile("s_memrealtime %0" : "=s"(ts_));
    u32 tstart, tprobe;
    extern __shared__ __attribute__((aligned(16))) char smem[];
    const u32 tid = threadIdx.x, lane = tid & 63, wave = __builtin_amdgcn_readfirstlane(tid >> 6), b = blockIdx.x;
    const u32 xcd = b & 7, slot = b >> 3;
    const u32 L0 = (u32)(size_t)(LAS char*)smem;
    u32 junk, junk2;
    auto code_pf = [&]() __attribute__((always_inline)) {
        asm volatile("s_getpc_b64 s[60:61]\n.Lh3_here:\n\ts_min_u32 %0, %2, .Lh3_end-.Lh3_here\n\ts_load_dword %1, s[60:61], %0 offset:64\n\ts_load_dword %0, s[60:61], %0"
                     : "=&s"(junk), "=&s"(junk2) : "s"((PF_ORDER ? slot * 8 + wave : wave * 32 + slot) * 128) : "scc", "s60", "s61", "memory");
    };
    if (CODE_PF == 1) code_pf();
    const u32 tt = H1SAME ? slot : xcd * 4 + (slot >> 3), cg = H1SAME ? xcd : slot & 7;
    const u32 grp = MAP48 ? (xcd >> 1) * 4 + (slot & 3) : xcd * 2 + (slot & 1);
    const u32 rank = MAP48 ? (xcd & 1) * 8 + (slot >> 2) : slot >> 1;
    const u32 lane16 = lane * 16;
    u32x4 wsk;
    const u16 *gq, *gk, *gv, *gsk, *gh, *gD, *gW, *gphi, *gwo; u16* gout; char* ws;
    {
        const u32 row = tid >> 3, c8 = tid & 7;
        const u32 g0 = (tt * 64 + row) * 1024 + cg * 128;
        u32 ok = g0 + ((c8 ^ (((row + 8) >> 2) & 7)) << 4), ov = g0 + ((c8 ^ ((row >> 2) & 7)) << 4);
        u32 oh = ((ov - 8192) | (tt == 0 ? OOB : 0u)) & 0xBFFFFFFFu, op = rank * 1024 + lane16, os = cg * 512 + (tid & ~15u);
        asm volatile("" : "+v"(ok), "+v"(ov), "+v"(oh), "+v"(op), "+v"(os));
        SB();
        asm volatile("s_waitcnt lgkmcnt(0)" : "+s"(ka), "+s"(kb), "+s"(kc), "+s"(ts_)); tstart = (u32)ts_; tprobe = tstart;
        gq = (const u16*)ka[0]; gk = (const u16*)ka[1]; gv = (const u16*)ka[2]; gsk = (const u16*)ka[3]; gh = (const u16*)ka[4]; gD = (const u16*)ka[5];
        gW = (const u16*)ka[6]; gphi = (const u16*)ka[7]; gwo = (const u16*)kb[0]; gout = (u16*)kb[1]; ws = (char*)kc;
        if (CODE_PF == 2) code_pf();
        const rsrc_t rk = mkrs(gk, S * D * 2), rv = mkrs(gv, S * D * 2);
        asm volatile("buffer_load_dwordx4 %0, %1, %2, 0 offen" : "=&v"(wsk) : "v"(os), "s"(mkrs(gsk, D * 4 * 2)) : "memory");
        dma(rk, ok, L0 + L_PK + (wave + 1) * 1024);
        dma(rv, ov, L0 + L_PV + wave * 1024);
        if (wave == 0) dma(rk, oh, L0 + L_PK);
        if (wave == 1) dma(mkrs(gphi, 32 * 256 * 2), op, L0 + L_PH);
    }
    asm volatile("s_barrier" ::: "memory");
    const rsrc_t rw = mkrs(ws, WS_BYTES);
    const u32 pair = xcd * 32 + slot;
    const u32 rho = lane & 31, hh = lane >> 5, g16 = lane >> 4, l16 = lane & 15;
    const u32 w4 = wave & 3, Td = grp * 4 + (rank >> 2), tile = (rank & 3) * 4 + w4;
    const u32x4 sent4 = {SENT, SENT, SENT, SENT};
    const u32 srow = grp * 128 + rank * 8 + w4 * 2 + hh;
    u32x4 sa[2], xrow[2], hA; u32x2 hB;
    {
        const bool isW = wave < 3, isC = wave < 4;
        const rsrc_t r = mkrs(isW ? gW : isC ? gwo : gq, isW ? D * 3 * D * 2 : isC ? D * D * 2 : S * D * 2);
        const u32 rs_ = isW ? 3072 : 1024;
        const u32 sb = (isC ? grp : Td) * 32 * rs_ + (isW ? wave * 1024 : 0) + (isC ? rank : tile) * 64;
        const u32 o = __umul24(lane >> 2, rs_) + sb + (((lane ^ (lane >> 3)) & 3) << 4);
        dma(r, o, L0 + L_SA + wave * 2048);
        dma(r, o + 16 * rs_, L0 + L_SA + wave * 2048 + 1024);
        const rsrc_t rq = mkrs(gq, isC ? S * D * 2 : 0);
#pragma unroll
        for (int i = 0; i < 2; ++i) xrow[i] = ld4<0>(rq, srow * 1024 + i * 512 + rho * 16, 0);
        const rsrc_t rh = mkrs(gh + (size_t)(pair * 2 + (wave >> 2)) * S, S * 2);
        hA = ld4<0>(rh, (tid & 255) * 16, 0);
        hB = __builtin_amdgcn_raw_buffer_load_b64(rh, (tid & 255) * 16 + 16, 0, 0);
    }
    fence();
    const u32 tagv = ld1<0>(rw, 0, WS_E);
    fence();
    if (wave >= 4) __builtin_amdgcn_s_setprio(3);
    u32 tag, ofl;
    {
        const u32 pp = tid >> 4, tq = tid & 15;
        const u32 cw = (pp & 3) * 4, pch = pp >> 2;
        u32 a1 = L0 + L_PK + (4 * tq + 4) * 128 + ((pch ^ ((tq + 1) & 7)) << 4) + cw;
        u32 a2 = L0 + L_PK + (4 * tq + 8) * 128 + ((pch ^ ((tq + 2) & 7)) << 4) + cw;
        u32 a3 = L0 + L_PV + (4 * tq) * 128 + ((pch ^ (tq & 7)) << 4) + cw;
        u32 okv = WS_KVP + (cg * 32 + pp) * 8192 + (tt * 64 + 4 * tq) * 4;
        const u32 m8 = lane < 8 ? 0u : OOB;
        ofl = (WS_F1 + ((cg * 8 + wave) * 32 + tt) * 128 + lane16) | m8;
        u32 oc1 = WS_YP + pair * 8192 + tid * 16, oc2 = WS_XF + (Td * 32 + 2 * tile + (wave >> 2)) * 1024 + lane16, oc3 = (WS_DS + (Td * 16 + tile) * 256 + (wave >> 2) * 128 + lane16) | m8;
        asm volatile("" : "+v"(a1), "+v"(a2), "+v"(a3), "+v"(okv), "+v"(ofl), "+v"(oc1), "+v"(oc2), "+v"(oc3));
        asm volatile("s_waitcnt vmcnt(9)" : "+v"(wsk) :: "memory");
        f32x2 w1[4], w2[4];
#pragma unroll
        for (int j = 0; j < 4; ++j) {
            const f32x2 w = (j & 1) ? f32x2{hi(wsk[j >> 1]), hi(wsk[2 + (j >> 1)])} : f32x2{lo(wsk[j >> 1]), lo(wsk[2 + (j >> 1)])};
            w1[j] = w * 65537.f; w2[j] = w * -65536.f;
            asm volatile("" : "+v"(w1[j]), "+v"(w2[j]));
        }
        u32x4 s4 = sent4;
        asm volatile("" : "+v"(s4));
        SB();
        asm volatile("s_waitcnt vmcnt(7)" ::: "memory");
        STAMP(1);
        bar();
        if (CODE_PF) asm volatile("" ::"s"(junk), "s"(junk2));
        u32 kk[7], vv[4];
#pragma unroll
        for (int i = 0; i < 3; ++i) kk[i] = lds_ld<u32>(a1 + (i + 1) * 128);
#pragma unroll
        for (int i = 0; i < 4; ++i) kk[3 + i] = lds_ld<u32>(a2 + i * 128);
#pragma unroll
        for (int i = 0; i < 4; ++i) vv[i] = lds_ld<u32>(a3 + i * 128);
        SB();
        st4<SC1>(s4, rw, oc1, 0);
        if (CX == 1) { st4<SC1>(s4, rw, oc2, 0); st4<SC1>(s4, rw, oc3, 0); }
        SB();
        f32x2 kf[7], sm[4];
#pragma unroll
        for (int i = 0; i < 7; ++i) kf[i] = up2(kk[i]);
#pragma unroll
        for (int i = 0; i < 4; ++i) sm[i] = pfma(kf[i], w2[3], kf[i] * w1[3]);
#pragma unroll
        for (int j = 2; j >= 0; --j)
#pragma unroll
            for (int i = 0; i < 4; ++i) sm[i] = rb2(sm[i] + pfma(kf[i + 3 - j], w2[j], kf[i + 3 - j] * w1[j]));
        u32x4 kv;
#pragma unroll
        for (int i = 0; i < 4; ++i) kv[i] = pk2(sm[i] * up2(vv[i]));
        st4<SC1>(kv, rw, okv, 0);
        SB();
        STAMP(2);
        __builtin_amdgcn_s_setprio(0);
        tag = __builtin_amdgcn_readfirstlane(tagv) + 1u;
        asm volatile("" ::"v"(xrow[0]), "v"(xrow[1]), "v"(hA), "v"(hB) : "memory");
        const u32 row = wave < 3 ? (rho & 0x13) | ((rho & 4) << 1) | ((rho & 8) >> 1) : wave == 3 ? (rho & 3) | ((rho & 4) << 2) | ((rho & 24) >> 1) : 4 * (rho & 7) + (rho >> 3);
        const u32 a = L0 + L_SA + wave * 2048 + row * 64 + (((hh ^ (row >> 1)) & 3) << 4);
        sa[0] = lds_ld<u32x4>(a); sa[1] = lds_ld<u32x4>(a ^ 32);
        if (CX == 2) { st4<SC1>(s4, rw, oc2, 0); st4<SC1>(s4, rw, oc3, 0); }
    }
    SB();
    const u32x4 tag4 = {tag, tag, tag, tag};
    if (wave < 4) {
        {
            u32x4 bf; u32 sel;
            if (wave < 2) {
                sel = (rho >> 2) & 1;
                const u32 a = L0 + L_PH + (lane & 1) * 512 + (hh * 8 + (l16 >> 2)) * 32 + ((g16 & 1) * 2 + ((lane >> 1) & 1)) * 8;
                const u32x2 p0 = lds_tr(a), p1 = lds_tr(a + 128);
                bf = u32x4{p0.x, p0.y, p1.x, p1.y};
            } else {
                const u32 n = wave < 3 ? (((rho >> 2) & 1) * 16 + (rho & 3) + 4 * (rho >> 3)) : ((rho & 0x13) | ((rho & 4) << 1) | ((rho & 8) >> 1));
                sel = n >> 4;
                const u32 e = (n & 15) - hh * 8;
                const u32 one = (e & 1) ? ONE_HI : ONE_LO;
                bf = u32x4{(e >> 1) == 0 ? one : 0u, (e >> 1) == 1 ? one : 0u, (e >> 1) == 2 ? one : 0u, (e >> 1) == 3 ? one : 0u};
            }
            const u32x4 z4 = {0, 0, 0, 0};
            f32x16 acc = {};
            acc = mfma32(sa[0], sel == 0 ? bf : z4, acc);
            acc = mfma32(sa[1], sel == 1 ? bf : z4, acc);
            const u32 o = (wave < 3 ? WS_WQ + ((rank * 3 + wave) * 32 + grp * 2) * 1024 : WS_WO + (rank * 32 + grp * 2) * 1024);
            float cs = 0.f;
#pragma unroll
            for (int s = 0; s < 2; ++s) {
                const u32x4 w = {pk(acc[8 * s], acc[8 * s + 1]), pk(acc[8 * s + 2], acc[8 * s + 3]), pk(acc[8 * s + 4], acc[8 * s + 5]), pk(acc[8 * s + 6], acc[8 * s + 7])};
                st4<SC1>(w, rw, lane16 + s * 1024, o);
#pragma unroll
                for (int i = 0; i < 4; ++i) cs = dot2(w[i], ONE2, cs);
            }
            st1<SC1>(uasf(halfsum(cs)), rw, lane < 32 ? WS_CS + ((rank * 4 + wave) * 16 + grp) * 128 + lane * 4 : OOB, 0);
        }
        STAMP(14);
        if (CX == 2) asm volatile("s_waitcnt vmcnt(5)" ::: "memory"); else asm volatile("s_waitcnt vmcnt(3)" ::: "memory");
        STAMP(32);
        st4<SC1>(tag4, rw, ofl, 0);
        SB();
        {
            float sum = 0.f, sq = 0.f;
#pragma unroll
            for (int j = 0; j < 8; ++j) { sum = dot2(xrow[j >> 2][j & 3], ONE2, sum); sq = dot2(xrow[j >> 2][j & 3], xrow[j >> 2][j & 3], sq); }
            sum = rowpairsum(dppadd<0x128>(dppadd<0x124>(dppadd<0x4E>(dppadd<0xB1>(sum)))));
            sq = rowpairsum(dppadd<0x128>(dppadd<0x124>(dppadd<0x4E>(dppadd<0xB1>(sq)))));
            float m, s; lnstat(sum, sq, m, s);
            st2<SC1>(u32x2{pk(m, s), tag}, rw, rho < 8 ? srow * 64 + rho * 8 : OOB, WS_ST0);
        }
        asm volatile("s_waitcnt vmcnt(2)" ::: "memory");
        st4<SC1>(tag4, rw, wave < 3 && lane < 8 ? WS_FW + ((rank * 16 + grp) * 4 + wave) * 128 + lane16 : OOB, 0);
        STAMP(3);
        if (tid < 180) { const u32 c = tid >= 90; lds_st<u32x4>(L0 + L_UR + c * UR_CH + 64 * 96 + (tid - c * 90) * 16, u32x4{0, 0, 0, 0}); }
    } else {
        STAMP(37);
        if (CX == 2) asm volatile("s_waitcnt vmcnt(2)" ::: "memory"); else drain();
        st4<SC1>(tag4, rw, ofl, 0);
        STAMP(36);
    }
    {
        const u32 hq = tid & 255;
        u32x4 a = hA; u32x2 c = hB; u32 p = L0 + L_HC + (tid >> 8) * HC_CH + (hq + 4) * 16;
#pragma unroll 1
        for (int it = 0; it < 2; ++it) {
            const u32 d[6] = {a.x, a.y, a.z, a.w, c.x, c.y};
            u32 al[5];
#pragma unroll
            for (int i = 0; i < 5; ++i) al[i] = __builtin_amdgcn_alignbit(d[i + 1], d[i], 16);
            lds_st<u32x4>(p, u32x4{al[0], al[1], al[2], al[3]});
            lds_st<u32x4>(p + HC_COPY, u32x4{d[1], d[2], d[3], d[4]});
            lds_st<u32x4>(p + 2 * HC_COPY, u32x4{al[1], al[2], al[3], al[4]});
            lds_st<u32x4>(p + 3 * HC_COPY, u32x4{d[2], d[3], d[4], d[5]});
            if (hq >= 4) break;
            c = hq == 0 ? u32x2{a.x, a.y} : u32x2{0, 0};
            a = u32x4{0, 0, 0, 0};
            p -= (2 * hq + 1) * 16;
        }
    }
    if (wave < 4) {
        u32 tid_c = tid;
        asm volatile("" : "+v"(tid_c));
        const u32 lane = tid_c & 63, lane16 = lane * 16, rho = lane & 31, hh = lane >> 5, g16 = lane >> 4, l16 = lane & 15;
        const u32 ch = wave >> 1, it = wave & 1;
        u32 aA = L0 + L_HC + ch * HC_CH + (l16 & 3) * HC_COPY + 2 * (16 * it + 8 * g16 + (l16 & 12));
        u32 aB = L0 + L_UR + ch * UR_CH + (15 - l16) * 96 + 16 * g16;
        u32 aY = L0 + L_YC + ch * YC_CH + l16 * 72 + (16 * it + 4 * g16) * 2;
        u32 f3o = wave == 0 ? WS_F3 + (b & 3) * 4096 + lane * 64 : OOB;
        f32x4 acc[4] = {};
        asm volatile("" : "+v"(aA), "+v"(aB), "+v"(aY), "+v"(f3o), "+v"(acc[0]), "+v"(acc[1]), "+v"(acc[2]), "+v"(acc[3]));
        STAMP(13);
        bar();
        STAMP(4);
        {
            asm volatile(
                ".macro H3MM d, a, u\n\tv_mfma_f32_16x16x32_bf16 \\d, v[\\a:\\a+3], v[\\u:\\u+3], \\d\n.endm\n"
                ".macro H3RA r, i, n, o\n\tds_read_b64 v[\\r+4*\\i+2*\\o:\\r+4*\\i+2*\\o+1], %4 offset:1024*\\i+64*\\n+8*\\o\n.endm\n"
                ".macro H3RU r, i, n\n\tds_read_b128 v[\\r+16+4*\\i:\\r+16+4*\\i+3], %5 offset:1536*(3-\\i)+96*\\n\n.endm\n"
                ".macro H3STEP x, y, n\n"
                "s_waitcnt lgkmcnt(6)\n"
                "H3MM %0, \\x, \\x+16\n H3RA \\y, 0, \\n, 0\n H3RA \\y, 0, \\n, 1\n"
                "H3MM %1, \\x, \\x+20\n H3RU \\y, 0, \\n\n"
                "H3MM %2, \\x, \\x+24\n H3RU \\y, 1, \\n\n"
                "H3MM %3, \\x, \\x+28\n H3RU \\y, 2, \\n\n"
                "s_waitcnt lgkmcnt(9)\n"
                "H3MM %1, \\x+4, \\x+16\n H3RU \\y, 3, \\n\n"
                "H3MM %2, \\x+4, \\x+20\n H3RA \\y, 1, \\n, 0\n H3RA \\y, 1, \\n, 1\n"
                "H3MM %3, \\x+4, \\x+24\n H3RA \\y, 2, \\n, 0\n"
                "s_waitcnt lgkmcnt(9)\n"
                "H3MM %2, \\x+8, \\x+16\n H3RA \\y, 2, \\n, 1\n"
                "H3MM %3, \\x+8, \\x+20\n H3RA \\y, 3, \\n, 0\n"
                "H3MM %3, \\x+12, \\x+16\n H3RA \\y, 3, \\n, 1\n"
                ".endm\n"
                "H3RA 64, 0, 0, 0\n H3RA 64, 0, 0, 1\n H3RU 64, 0, 0\n H3RU 64, 1, 0\n H3RU 64, 2, 0\n H3RU 64, 3, 0\n"
                "H3RA 64, 1, 0, 0\n H3RA 64, 1, 0, 1\n H3RA 64, 2, 0, 0\n H3RA 64, 2, 0, 1\n H3RA 64, 3, 0, 0\n H3RA 64, 3, 0, 1\n"
                "s_mov_b32 s60, 4\n"
                ".Lh3_conv:\n"
                "H3STEP 64, 96, 1\n"
                "H3STEP 96, 64, 2\n"
                "H3STEP 64, 96, 3\n"
                "s_sub_u32 s60, s60, 1\n"
                "H3STEP 96, 64, 4\n"
                "v_add_u32 %4, 0x100, %4\n v_add_u32 %5, 0x180, %5\n"
                "s_cmp_lg_u32 s60, 0\n"
                "s_cbranch_scc1 .Lh3_conv\n"
                "s_waitcnt lgkmcnt(0)\n s_nop 7\n"
                ".purgem H3MM\n.purgem H3RA\n.purgem H3RU\n.purgem H3STEP\n"
                : "+v"(acc[0]), "+v"(acc[1]), "+v"(acc[2]), "+v"(acc[3]), "+v"(aA), "+v"(aB)
                :
                : "memory", "scc", "s60", "v64", "v65", "v66", "v67", "v68", "v69", "v70", "v71", "v72", "v73", "v74", "v75", "v76", "v77", "v78", "v79",
                  "v80", "v81", "v82", "v83", "v84", "v85", "v86", "v87", "v88", "v89", "v90", "v91", "v92", "v93", "v94", "v95",
                  "v96", "v97", "v98", "v99", "v100", "v101", "v102", "v103", "v104", "v105", "v106", "v107", "v108", "v109", "v110", "v111",
                  "v112", "v113", "v114", "v115", "v116", "v117", "v118", "v119", "v120", "v121", "v122", "v123", "v124", "v125", "v126", "v127");
#pragma unroll
            for (int c = 0; c < 4; ++c) lds_st<u32x2>(aY + 16 * 72 * c, u32x2{pk(acc[c][0], acc[c][1]), pk(acc[c][2], acc[c][3])});
        }
        STAMP(5);
        u32x4 f3[2];
        f3[0] = ld4<SC1>(rw, f3o, 0); f3[1] = ld4<SC1>(rw, f3o + 16, 0);
        bar();
        if (wave == 0) {
            const u32 tyc = now();
            u32 sp = 0;
            while (again(__any(((f3[0].y ^ tag) | (f3[0].w ^ tag) | (f3[1].y ^ tag) | (f3[1].w ^ tag)) != 0), sp)) {
                asm volatile("" : "+v"(f3o) :: "memory");
                f3[0] = ld4<SC1>(rw, f3o, 0); f3[1] = ld4<SC1>(rw, f3o + 16, 0);
            }
            fence();
            COUNT(27, sp);
            STAMP(23);
            const u32 own = __builtin_amdgcn_readlane((b & 2) ? ((b & 1) ? f3[1].z : f3[1].x) : ((b & 1) ? f3[0].z : f3[0].x), b >> 2);
            int lag = rowmax(max(max((int)(f3[0].x - own), (int)(f3[0].z - own)), max((int)(f3[1].x - own), (int)(f3[1].z - own))));
            lag = max(max(__builtin_amdgcn_readlane(lag, 0), __builtin_amdgcn_readlane(lag, 16)), max(__builtin_amdgcn_readlane(lag, 32), __builtin_amdgcn_readlane(lag, 48)));
            lag = min(lag, 1000) + Y_WAIT;
            COUNT(33, lag);
            wait_until(tyc, lag);
        }
        STAMP(25);
        bar();
        const u32 T = grp * 4 + wave;
        const u32 aW = L0 + L_W + lane16;
        u32 aW1 = aW + 33 * 1024, aW2 = aW + 66 * 1024;
        u32 aI = L0 + L_IMG + (wave * 32 + rho) * IMG_RS + hh * 64, aLM = L0 + L_LM + (wave * 32 + rho) * 16;
        u32 aN = ((rho >> 2) & 1) == hh ? L0 + L_KVT + (hh * 16 + (rho & 3) + 4 * (rho >> 3)) * KVT_RS : L0 + L_ZERO;
        u32 aZ = (l16 >> 2) == g16 ? L0 + L_Z + (g16 >> 1) * 64 : L0 + L_ZERO;
        u32 oo = WS_OF + (T * 32 + 2 * rank) * 1024 + lane16;
        asm volatile("" : "+v"(aW1), "+v"(aW2), "+v"(aI), "+v"(aLM), "+v"(aN), "+v"(aZ), "+v"(oo));
#pragma unroll 1
        for (int layer = 0; layer < 2; ++layer) {
            delay<2>();
            st4<SC1>(sent4, rw, oo, 0); st4<SC1>(sent4, rw, oo + 1024, 0);
            drain();
            STAMP(34);
            bar();
            STAMP(6 + 10 * layer);
            delay<DLY_X>();
            const u32 xb = WS_XF + layer * MB2 + T * 32768;
            u32 xo = lane16;
            u32x4 X[32], xm; f32x2 rs2;
            f32x16 aK, aV;
            u32 tries = 0, budget = SPIN_CAP; bool bad;
            do {
                asm volatile("" : "+v"(xo) :: "memory");
#pragma unroll
                for (int i = 0; i < XWIN; ++i) { X[i] = ld4<SC1>(rw, xo + (i & 3) * 1024, xb + (i >> 2) * 4096); if (i < XWIN - 1) delay<XGAP>(); SB(); }
                if (tries == 0) asm volatile("s_barrier" ::: "memory");
                aK = f32x16{}; aV = f32x16{};
                u32x4 wk[3], wv[3];
                constexpr int MS = BS_AT + 4;
                auto fr = [](int j) { return j < MS ? j : j == MS ? 32 : j - 1; };
#pragma unroll
                for (int j = 0; j < 2; ++j) { wk[j] = lds_ld<u32x4>(aW1 + j * 1024); wv[j] = lds_ld<u32x4>(aW2 + j * 1024); }
                SB();
                if (XFIX) xfix<0>(X[0], X[1], rw, xo, xb, budget);
                SB();
#pragma unroll
                for (int j = 0; j < 33; ++j) {
                    const int i = fr(j);
                    asm volatile("" ::"v"(wv[j % 3]));
                    SB();
                    aK = mfma32(wk[j % 3], i < 32 ? X[i & 31] : xm, aK);
                    if (j == 0) STAMP(24 + 16 * (1 - layer)); if (j == 20) STAMP(26 + 16 * (1 - layer));
                    SB();
                    if (j == BS_AT + 1) { xm = lds_ld<u32x4>(aLM); rs2 = lds_ld<f32x2>((aLM >> 1) + (L_LR - L_LM / 2) + L0 / 2); }
                    if (j < 31) wk[(j + 2) % 3] = lds_ld<u32x4>(aW1 + fr(j + 2) * 1024);
                    if (j + XWIN < 32) X[(j + XWIN) & 31] = ld4<SC1>(rw, xo + ((j + XWIN) & 3) * 1024, xb + ((j + XWIN) >> 2) * 4096);
                    SB();
                    aV = mfma32(wv[j % 3], i < 32 ? X[i & 31] : xm, aV);
                    SB();
                    if (XFIX && j < 32 && fr(j + 1) < XFIX_N && (fr(j + 1) & 1) == 0) {
                        const int n = fr(j + 1) & 31;
                        if ((n & 2) == 0) xfix<0>(X[n], X[(n + 1) & 31], rw, xo, xb + (n >> 2) * 4096, budget);
                        else xfix<2048>(X[n], X[(n + 1) & 31], rw, xo, xb + (n >> 2) * 4096, budget);
                    }
                    SB();
                    if (j == BS_AT && tries == 0) asm volatile("s_barrier" ::: "memory");
                    if (j < 31) wv[(j + 2) % 3] = lds_ld<u32x4>(aW2 + fr(j + 2) * 1024);
                    SB();
                    SB();
                }
                bad = __any(aK[0] != aK[0]);
                if (RARE(bad)) {
                    u32 m = 0;
#pragma unroll
                    for (int i = 0; i < 32; ++i) m = max4(m, X[i]);
                    bad = __any(m == SENT) && ++tries < 4096;
                }
            } while (RARE(bad));
            COUNT(28 + layer, tries * 1000 + SPIN_CAP - budget);
            STAMP(7 + 10 * layer);
            float rs = rs2.x, c = rs2.y;
            asm volatile("" : "+v"(rs), "+v"(c));
            u32x4 phi[4];
            softmax16(aK, c, -max16(aK) * c, phi[0], phi[1]);
            const float mn = min16(aK) * c;
            lds_st<u32x4>(aI, phi[0]); lds_st<u32x4>(aI + 16, phi[1]);
            SB();
            lds_st<u32x4>(aI + 128 - hh * 32, pk8(aV, 0, rs)); lds_st<u32x4>(aI + 144 - hh * 32, pk8(aV, 1, rs));
            SB();
            softmax16(aK, -c, mn, phi[2], phi[3]);
            lds_st<u32x4>(aI + 32, phi[2]); lds_st<u32x4>(aI + 48, phi[3]);
            STAMP(8 + 10 * layer);
            bar();
            delay<DLY_P2>();
            f32x16 aQ = {};
            {
                u32x4 wq[4];
#pragma unroll
                for (int i = 0; i < 3; ++i) wq[i] = lds_ld<u32x4>(aW + i * 1024);
                SB();
#pragma unroll
                for (int i = 0; i < 33; ++i) {
                    if (i + 3 < 33) wq[(i + 3) & 3] = lds_ld<u32x4>(aW + (i + 3) * 1024);
                    aQ = mfma32(wq[i & 3], i < 32 ? X[i & 31] : xm, aQ);
                    SB();
                }
            }
            asm volatile("s_nop 11" : "+v"(aQ));
            softmax16(aQ, c, -max16(aQ) * c, phi[0], phi[1]);
            softmax16(aQ, -c, min16(aQ) * c, phi[2], phi[3]);
            asm volatile("" : "+v"(phi[0]), "+v"(phi[1]), "+v"(phi[2]), "+v"(phi[3]));
            STAMP(9 + 10 * layer);
            bar();
            STAMP(10 + 10 * layer);
            {
                u32x4 zf[4], nf[4];
#pragma unroll
                for (int s = 0; s < 4; ++s) zf[s] = lds_ld<u32x4>(aZ + s * 16);
#pragma unroll
                for (int s = 0; s < 4; ++s) nf[s] = lds_ld<u32x4>(aN + s * 16);
                SB();
                f32x16 num = {}; f32x4 den = {};
#pragma unroll
                for (int s = 0; s < 4; ++s) den = mfma16(zf[s], phi[s], den);
#pragma unroll
                for (int s = 0; s < 4; ++s) num = mfma32(nf[s], phi[s], num);
                const float r = __builtin_amdgcn_rcpf(den[0] + 1e-6f);
#pragma unroll
                for (int s = 0; s < 2; ++s) st4<SC1>(nos4(pk8(num, s, r)), rw, oo + s * 1024, 0);
            }
            STAMP(11 + 10 * layer);
            if (O_DMA) {
                delay<DLY_O>();
                dma8k(rw, lane16, WS_OF + ((layer * 64 + Td) * 32 + wave * 8) * 1024, L0 + L_O + wave * 8192);
                drain();
            } else {
                bar();
                delay<DLY_O>();
                const u32 sp = o_gather(rw, lane16, WS_OF + ((layer * 64 + Td) * 32 + wave * 4) * 1024, L0 + L_O + wave * 4096 + lane16);
                COUNT(30 + layer, sp);
            }
            STAMP(12 + 10 * layer);
            bar();
            if (O_DMA) {
                u32 sp = 0; bool bad;
                do {
                    fence();
                    u32 m = 0;
#pragma unroll
                    for (int i = 0; i < 8; ++i) m = max4(m, lds_ld<u32x4>(L0 + L_O + wave * 8192 + i * 1024 + lane16));
                    bad = again(__any(m == SENT), sp);
                    if (RARE(bad)) { dma8k(rw, lane16, WS_OF + ((layer * 64 + Td) * 32 + wave * 8) * 1024, L0 + L_O + wave * 8192); drain(); }
                } while (RARE(bad));
                if (lane == 0) __hip_atomic_fetch_add((LAS u32*)(size_t)(L0 + L_OK), 1u, __ATOMIC_RELAXED, __HIP_MEMORY_SCOPE_WORKGROUP);
            }
            oo += MB2;
        }
        DUMP();
        asm volatile(".subsection 2\n.Lh3_end:\n\t.fill 64, 4, 0xBF800000\n.subsection 0");
        __builtin_amdgcn_endpgm();
    } else {
        __builtin_amdgcn_s_setprio(3);
        const u32 v = wave - 4, dt = tid - 256, T = Td;
        const u32 dD = *(const __attribute__((address_space(4))) u32*)(gD + 2 * pair);
        const u32 tok = T * 32 + 4 * (rho & 7) + (rho >> 3);
        const u32 xoff = WS_XF + (T * 32 + 2 * tile) * 1024 + lane16;
        const u32 dpos = l16 * 16 + g16 * 4, dslot = WS_DS + (T * 16 + tile) * 256;
        u32x4 xr[2] = {sa[0], sa[1]};
        const u32 so = (WS_SP + (rank * 16 + grp) * SP_UNIT + v * SP_WAVE + lane16) | (lane < 40 ? 0u : OOB);
        auto clear2 = [&]() __attribute__((always_inline)) {
            st4<SC1>(sent4, rw, so + 256 * SP_UNIT, 0);
            st4<SC1>(sent4, rw, xoff + MB2, 0); st4<SC1>(sent4, rw, xoff + MB2 + 1024, 0);
            st4<SC1>(sent4, rw, (dslot + (1 + g16) * DS_BUF + l16 * 16) | (lane < 32 ? 0u : OOB), 0);
            fence();
        };
        u32x4 R[32];
        u32 cp[6];
        f32x2 xf[8], b1[8], b2[8];
        {
            const u32 qoff = pair * 8192 + v * 2048 + lane16;
            u32x4 kv[2]; u32 au[2], ay[2];
#pragma unroll
            for (int i = 0; i < 2; ++i) {
                const u32 t0 = 4 * (v * 128 + i * 64 + lane), i0 = 2044 - t0;
                au[i] = L0 + L_UR + (i0 >> 5) * 96 + (i0 & 31) * 2; ay[i] = L0 + L_YC + (t0 >> 5) * 72 + (t0 & 31) * 2;
            }
            f32x2 d1 = up2(dD) * 65537.f, d2 = up2(dD) * -65536.f;
            u32 selLo = ONE_LO, selHi = ONE_HI, oy = WS_YP + qoff, ok = WS_KVP + qoff;
            u32 fo = WS_FW + ((rank * 16 + v * 4) * 4 + ((l16 & 3) == 3 ? 0 : l16)) * 128;
            u32 o3 = dt < 4 ? WS_F3 + lane * 4096 + (b >> 2) * 64 + (b & 3) * 8 : OOB;
            asm volatile("" : "+v"(o3));
            asm volatile("" : "+s"(selLo), "+s"(selHi), "+v"(au[0]), "+v"(au[1]), "+v"(ay[0]), "+v"(ay[1]), "+v"(oy), "+v"(ok), "+v"(fo), "+v"(d1), "+v"(d2));
            delay<DLY_F1>();
            { const u32 sp = wait_flags(rw, WS_F1 + ((pair >> 2) * 32 + v * 8 + (lane & 7)) * 128, tag); COUNT(26, sp); }
            STAMP(4);
#pragma unroll
            for (int i = 0; i < 2; ++i) kv[i] = ld4<SC1>(rw, ok + i * 1024, 0);
            const u32 fw = ld1<SC1>(rw, fo, 0);
            SB();
#pragma unroll
            for (int i = 0; i < 2; ++i) {
                lds_st<u32x2>(au[i], u32x2{__builtin_amdgcn_perm(kv[i].z, kv[i].w, 0x05040100u), __builtin_amdgcn_perm(kv[i].x, kv[i].y, 0x05040100u)});
                lds_st<u32x2>(au[i] + UR_CH, u32x2{__builtin_amdgcn_perm(kv[i].z, kv[i].w, 0x07060302u), __builtin_amdgcn_perm(kv[i].x, kv[i].y, 0x07060302u)});
            }
            f32x2 tD[8];
#pragma unroll
            for (int i = 0; i < 8; ++i) { const f32x2 k2 = up2(kv[i >> 2][i & 3]); tD[i] = pfma(k2, d2, k2 * d1); asm volatile("" : "+v"(tD[i])); }
            STAMP(5);
            bar();
            st2<SC1>(u32x2{now(), tag}, rw, o3, 0);
            st4<SC1>(sent4, rw, so, 0);
            COUNT(42, 1);
            if (RARE(__any(fw != tag))) { wait_flags(rw, fo, tag); COUNT(42, 0); }
            fence();
            u32x2 st0 = __builtin_amdgcn_raw_buffer_load_b64(rw, tok * 64, WS_ST0, SC1);
#pragma unroll
            for (int i = 0; i < 6; ++i) cp[i] = ld1<SC1>(rw, WS_CS + (rank * 64 + v * 4 + hh * 2) * 128 + rho * 4 + (i & 1) * 128, (i >> 1) * 2048);
#pragma unroll
            for (int i = 0; i < 24; ++i) R[i] = ld4<SC1>(rw, lane16 + (i & 3) * 1024, WS_WQ + ((rank * 3 + (i >> 3)) * 32 + v * 8 + (i & 4)) * 1024);
            STAMP(34);
            if (CL == 1) clear2();
            if (CL == 1) asm volatile("s_waitcnt vmcnt(35)" ::: "memory"); else asm volatile("s_waitcnt vmcnt(31)" ::: "memory");
            STAMP(14);
            bar();
            STAMP(15);
            u32x2 ya[2], yb[2];
#pragma unroll
            for (int i = 0; i < 2; ++i) { ya[i] = lds_ld<u32x2>(ay[i]); yb[i] = lds_ld<u32x2>(ay[i] + YC_CH); }
#pragma unroll
            for (int i = 0; i < 2; ++i) {
                u32x4 y;
#pragma unroll
                for (int e = 0; e < 4; ++e) {
                    const u32 sel = (e & 1) ? selHi : selLo;
                    y[e] = nos(pk(dot2(ya[i][e >> 1], sel, tD[4 * i + e].x), dot2(yb[i][e >> 1], sel, tD[4 * i + e].y)));
                }
                st4<SC1>(y, rw, oy + i * 1024, 0);
            }
            SB();
            STAMP(23);
            if (RARE(__any(st0.y != tag))) {
                u32 sp = 0;
                do { fence(); st0 = __builtin_amdgcn_raw_buffer_load_b64(rw, tok * 64, WS_ST0, SC1); } while (again(__any(st0.y != tag), sp));
            }
            const float rs = __builtin_amdgcn_rcpf(hi(st0.x)) * 1.0000003f, mean = lo(st0.x);
#pragma unroll
            for (int i = 0; i < 8; ++i) {
                xf[i] = up2(xr[i >> 2][i & 3]);
                const f32x2 n = rb2(rb2(xf[i] - mean) * rs);
                b1[i] = n * 65537.f; b2[i] = n * -65536.f;
                asm volatile("" : "+v"(xf[i]), "+v"(b1[i]), "+v"(b2[i]));
            }
        }
        u32x4 yt[2];
        u32 poff = WS_YP + ((tile * 16 + (lane >> 3)) * 2048 + T * 32 + 4 * (lane & 7)) * 4;
        u32 aWs = L0 + L_W + v * 8192 + lane16;
        asm volatile("" : "+v"(poff), "+v"(aWs));
        auto wo_load = [&](int i0, int i1) __attribute__((always_inline)) {
#pragma unroll
            for (int i = i0; i < i1; ++i) R[i] = ld4<SC1>(rw, lane16 + (i & 3) * 1024, WS_WO + (tile * 32 + (i & ~3)) * 1024);
        };
        STAMP(25);
        bar();
#pragma unroll
        for (int s = 0; s < 2; ++s) yt[s] = ld4<SC1>(rw, poff, s * 65536);
        SB();
        delay<2>();
        if (WO_W2 >= 1) wo_load(24, 32);
        SB();
#pragma unroll
        for (int i = 8; i < 16; ++i) lds_st<u32x4>(aWs + (33 + (i & 7)) * 1024, R[i]);
        SB();
        if (WO_W2 >= 2) wo_load(8, 16);
        if (CL == 3) clear2();
        SB();
        COUNT(41, 0);
        if (RARE(__any(max4(max4(0, yt[0]), yt[1]) == SENT))) {
            u32 sp = 0;
            do { fence(); yt[0] = ld4<SC1>(rw, poff, 0); yt[1] = ld4<SC1>(rw, poff, 65536); } while (again(__any(max4(max4(0, yt[0]), yt[1]) == SENT), sp));
            COUNT(41, sp + 1);
        }
        STAMP(13);
#pragma unroll
        for (int s = 0; s < 2; ++s) {
            yt[s] = t1(yt[s]);
#pragma unroll
            for (int i = 0; i < 4; ++i) { const f32x2 y = up2(yt[s][i]); xr[s][i] = nos(pk2(xf[4 * s + i] + pfma(y, b2[4 * s + i], y * b1[4 * s + i]))); }
            st4<SC1>(xr[s], rw, xoff + s * 1024, 0);
        }
        SB();
        STAMP(3);
        u32 tb4;
        auto at_b4 = [&](u32 layer) __attribute__((always_inline)) {
            bar();
            stats_publish(xr, rw, dpos, dslot + 2 * layer * DS_BUF);
            tb4 = now();
            if (TS_PACE) st4<SC1>(u32x4{tb4, tag, tb4, tag}, rw, dt < 8 ? WS_TS + ((layer * 16 + rank) * 16 + grp) * 128 + lane16 : OOB, 0);
            SB();
        };
        u32x2 ts;
        auto ln_to_lds = [&](u32 layer, auto meanwhile) __attribute__((always_inline)) {
            const u32 off = WS_DS + 2 * layer * DS_BUF + (grp * 4 + v) * 4096;
            u32x4 ds[4]; float t, mean, rs;
            meanwhile();
            SB();
            wait_until(tb4, SC_WAIT);
            stats_issue(ds, rw, lane16, off);
            STAMP(45 + layer);
            bar();
            const u32 sp = stats_check(ds, rw, lane16, off, t);
            COUNT(21, sp);
            stats_finish(t, mean, rs);
            const u32 nm = pk(-mean, -mean);
            lds_st<u32x4>(L0 + L_LM + (v * 32 + rho) * 16, u32x4{nm, nm, nm, nm});
            lds_st<f32x2>(L0 + L_LR + (v * 32 + rho) * 8, f32x2{rs, rs * 1.44269504088896f});
            STAMP(7 + 10 * layer);
        };
        at_b4(0);
        aWs += 66 * 1024;
        asm volatile("" : "+v"(aWs));
#pragma unroll
        for (int i = 16; i < 24; ++i) lds_st<u32x4>(aWs + (i & 7) * 1024, R[i]);
        ln_to_lds(0, [&]() __attribute__((always_inline)) {
#pragma unroll
            for (int i = 0; i < 8; ++i) lds_st<u32x4>(L0 + L_W + v * 8192 + lane16 + i * 1024, R[i]);
#pragma unroll
            for (int p = 0; p < 3; ++p) {
                const float cs = halfsum(fasu(cp[2 * p]) + fasu(cp[2 * p + 1]));
                const float c0 = rb(cs), c1 = rb(cs - c0), c2 = cs - c0 - c1;
                lds_st<u32x2>(L0 + L_W + (p * 33 + 32) * 1024 + ((v >> 1) * 32 + rho) * 16 + (v & 1) * 8, u32x2{pk(c0, c1), pk(c2, 0.f)});
            }
            if (dt < 5) lds_st<u32x4>(dt < 4 ? L0 + L_ZERO + lane16 : L0 + L_OK, u32x4{0, 0, 0, 0});
        });
        const u32 head = v >> 1, fh = v & 1;
        u32 aO = L0 + L_O + lane16;
        u32 aP = L0 + L_IMG + (4 * g16 + (l16 >> 2)) * IMG_RS + (l16 & 3) * 8 + head * 64 + fh * 32, aV_ = aP + 128 - head * 32 - fh * 32;
        u32 spw = WS_SP + (rank * 16 + grp) * SP_UNIT + v * SP_WAVE + l16 * 32 + g16 * 8, spz = l16 == 0 ? spw + 512 + g16 * 8 : OOB;
        u32 spr = WS_SP + rank * 16 * SP_UNIT + (g16 >> 1) * SP_UNIT + v * SP_WAVE + l16 * 32 + (g16 & 1) * 16;
        u32 spy = WS_SP + rank * 16 * SP_UNIT + g16 * SP_UNIT + v * SP_WAVE + 512 + l16 * 4;
        u32 aK_ = L0 + L_KVT + (head * 16 + l16) * KVT_RS + fh * 32 + g16 * 8, aZ_ = L0 + L_Z + head * 64 + fh * 32 + l16 * 2;
        u32x4 idm;
        {
            const u32 e = l16 & 7, one = (l16 >> 3) == (g16 & 1) ? ((e & 1) ? ONE_HI : ONE_LO) : 0u;
            idm = u32x4{(e >> 1) == 0 ? one : 0u, (e >> 1) == 1 ? one : 0u, (e >> 1) == 2 ? one : 0u, (e >> 1) == 3 ? one : 0u};
        }
        asm volatile("" : "+v"(aO), "+v"(aP), "+v"(aV_), "+v"(spw), "+v"(spz), "+v"(spr), "+v"(spy), "+v"(aK_), "+v"(aZ_), "+v"(idm));
        auto at_bs = [&](u32 layer) __attribute__((always_inline)) {
            bar();
            if (TS_PACE) ts = __builtin_amdgcn_raw_buffer_load_b64(rw, WS_TS + ((layer * 16 + rank) * 16 + l16) * 128, 0, SC1);
        };
        at_bs(0);
        if (CL == 2) clear2();
        wo_load(0, 8);
        if (WO_W2 < 2) wo_load(8, 16);
        wo_load(16, 24);
        if (WO_W2 < 1) wo_load(24, 32);
        if (CL == 2) asm volatile("s_waitcnt vmcnt(%c0)" ::"i"(32 - 8 * WO_W2) : "memory");
        STAMP(35);
#pragma unroll 1
        for (int layer = 0; layer < 2; ++layer) {
            u32x4 ones = {ONE2, ONE2, ONE2, ONE2};
            asm volatile("" : "+v"(aO), "+v"(aP), "+v"(aV_), "+v"(spw), "+v"(spz), "+v"(spr), "+v"(spy), "+v"(aK_), "+v"(aZ_), "+v"(idm), "+v"(ones));
            bar();
            STAMP(6 + 10 * layer);
            {
                u32x2 p[8], q[8];
#pragma unroll
                for (int ks = 0; ks < 4; ++ks) {
                    p[2 * ks] = lds_tr(aP + ks * 32 * IMG_RS); p[2 * ks + 1] = lds_tr(aP + (ks * 32 + 16) * IMG_RS);
                    q[2 * ks] = lds_tr(aV_ + ks * 32 * IMG_RS); q[2 * ks + 1] = lds_tr(aV_ + (ks * 32 + 16) * IMG_RS);
                }
                SB();
                f32x4 kvp = {}, zp = {};
#pragma unroll
                for (int ks = 0; ks < 4; ++ks) {
                    const u32x4 pa = {p[2 * ks].x, p[2 * ks].y, p[2 * ks + 1].x, p[2 * ks + 1].y};
                    kvp = mfma16(pa, u32x4{q[2 * ks].x, q[2 * ks].y, q[2 * ks + 1].x, q[2 * ks + 1].y}, kvp);
                    zp = mfma16(pa, ones, zp);
                }
                st2<SC1>(u32x2{nos(pk(kvp[0], kvp[1])), nos(pk(kvp[2], kvp[3]))}, rw, spw, 0);
                st4<SC1>(nos4(__builtin_bit_cast(u32x4, zp)), rw, spz, 0);
            }
            SB();
            const u32 tsp = now();
            STAMP(layer ? 39 : 38);
            int lag = 0;
            if (TS_PACE) {
                if (RARE(__any(ts.y != tag))) {
                    u32 sp = 0;
                    do { fence(); ts = __builtin_amdgcn_raw_buffer_load_b64(rw, WS_TS + ((layer * 16 + rank) * 16 + l16) * 128, 0, SC1); } while (again(__any(ts.y != tag), sp));
                }
                lag = min(__builtin_amdgcn_readfirstlane(rowmax((int)(ts.x - tb4))), 1000);
            }
            COUNT(43 + layer, lag);
            wait_until(tsp, lag + ST_WAIT);
            {
                u32 sp = 0; bool bad; f32x4 t; float z;
                do {
                    asm volatile("" : "+v"(spr), "+v"(spy) :: "memory");
                    u32x4 pt[8]; u32 zt[4];
#pragma unroll
                    for (int i = 0; i < 4; ++i) zt[i] = ld1<SC1>(rw, spy, 4 * i * SP_UNIT);
#pragma unroll
                    for (int i = 0; i < 8; ++i) pt[i] = ld4<SC1>(rw, spr, 2 * i * SP_UNIT);
                    SB();
                    float za = fasu(zt[0]) + fasu(zt[1]), zb = fasu(zt[2]) + fasu(zt[3]);
                    asm volatile("" : "+v"(za), "+v"(zb));
                    z = halfsum(rowpairsum(za + zb));
                    lds_st<u16>(aZ_, (u16)pk(z, z));
                    SB();
                    t = f32x4{};
#pragma unroll
                    for (int i = 0; i < 8; ++i) t = mfma16(idm, pt[i], t);
                    lds_st<u32x2>(aK_, u32x2{pk(t[0], t[1]), pk(t[2], t[3])});
                    SB();
                    const float chk = t[0] + z;
                    bad = __any(chk != chk);
                    if (RARE(bad)) {
                        u32 m = max(max(zt[0], zt[1]), max(zt[2], zt[3]));
#pragma unroll
                        for (int i = 0; i < 8; ++i) m = max4(m, pt[i]);
                        bad = __any(m == SENT);
                    }
                } while (again(bad, sp));
                COUNT(28 + layer, sp);
            }
            STAMP(8 + 10 * layer);
            bar();
            f32x16 acc, res;
#pragma unroll
            for (int i = 0; i < 8; ++i) { res[2 * i] = lo(xr[i >> 2][i & 3]); res[2 * i + 1] = hi(xr[i >> 2][i & 3]); }
            asm volatile("" : "+v"(res));
            u32 spo = 0;
            if (!O_DMA) {
                bar();
                delay<DLY_O>();
                spo = o_gather(rw, lane16, WS_OF + ((layer * 64 + T) * 32 + wave * 4) * 1024, aO + wave * 4096);
            }
            bar();
            STAMP(9 + 10 * layer);
            {
                u32 sp = 0; bool bad;
                do {
                    u32x4 of[4];
#pragma unroll
                    for (int i = 0; i < 3; ++i) of[i] = lds_ld<u32x4>(aO + i * 1024);
                    SB();
#pragma unroll
                    for (int i = 0; i < 32; ++i) {
                        if (i + 3 < 32) of[(i + 3) & 3] = lds_ld<u32x4>(aO + (i + 3) * 1024);
                        acc = mfma32(R[i], of[i & 3], i == 0 ? res : acc);
                        SB();
                    }
                    bad = O_DMA && __any(acc[0] != acc[0]);
                    if (RARE(bad)) {
                        bad = sp == 0;
                        sp = 1;
                        u32 sq = 0;
                        do { fence(); } while (again(lds_ld<u32>(L0 + L_OK) < 4u * (layer + 1), sq));
                        fence();
                    }
                } while (RARE(bad));
                COUNT(30 + layer, sp + spo);
            }
#pragma unroll
            for (int i = 0; i < 8; ++i) xr[i >> 2][i & 3] = pk(acc[2 * i], acc[2 * i + 1]);
            STAMP(10 + 10 * layer);
            if (layer == 1) {
                const rsrc_t ro = mkrs(gout, S * D * 2);
                const u32 roff = (tok * D + tile * 32 + hh * 8) * 2;
                st4<0>(xr[0], ro, roff, 0);
                st4<0>(xr[1], ro, roff + 32, 0);
                STAMP(22);
                break;
            }
            stats_publish(xr, rw, dpos, dslot + DS_BUF);
            SB();
            asm volatile("" : "+v"(yt[0]), "+v"(yt[1]));
#pragma unroll
            for (int i = 0; i < 8; ++i) {
                xf[i] = up2(xr[i >> 2][i & 3]);
                const f32x2 y = up2(yt[i >> 2][i & 3]);
                b1[i] = y * 65537.f; b2[i] = y * -65536.f;
                asm volatile("" : "+v"(xf[i]), "+v"(b1[i]), "+v"(b2[i]));
            }
            spw += 256 * SP_UNIT; spz += 256 * SP_UNIT; spr += 256 * SP_UNIT; spy += 256 * SP_UNIT;
            delay<DLY_DS>();
            float mean, rs;
            { u32x4 ds[4]; float t; stats_issue(ds, rw, lane16, WS_DS + DS_BUF + T * 4096); const u32 sp = stats_check(ds, rw, lane16, WS_DS + DS_BUF + T * 4096, t); COUNT(24, sp); stats_finish(t, mean, rs); }
            STAMP(11);
#pragma unroll
            for (int s = 0; s < 2; ++s) {
#pragma unroll
                for (int i = 0; i < 4; ++i) {
                    const f32x2 n = rb2(rb2(xf[4 * s + i] - mean) * rs);
                    xr[s][i] = nos(pk2(xf[4 * s + i] + pfma(n, b2[4 * s + i], n * b1[4 * s + i])));
                }
                st4<SC1>(xr[s], rw, xoff + MB2 + s * 1024, 0);
            }
            SB();
            STAMP(12);
            at_b4(1);
            ln_to_lds(1, []() {});
            at_bs(1);
        }
        st1<0>(tag, rw, b == 0 && tid == 256 ? WS_E : OOB, 0);
        DUMP();
        __builtin_amdgcn_endpgm();
    }
}

extern "C" void kernel_launch(void* const* d_in, const int* in_sizes, int n_in, void* d_out, int out_size, void* d_ws, size_t ws_size, hipStream_t stream) {
    (void)hipFuncSetAttribute((const void*)h3_hedgehog_kernel, hipFuncAttributeMaxDynamicSharedMemorySize, LDS_BYTES);
    h3_hedgehog_kernel<<<256, 512, LDS_BYTES, stream>>>((const u16*)d_in[0], (const u16*)d_in[1], (const u16*)d_in[2], (const u16*)d_in[3], (const u16*)d_in[4],
                                                        (const u16*)d_in[5], (const u16*)d_in[7], (const u16*)d_in[8], (const u16*)d_in[9], (u16*)d_out, (char*)d_ws);
}
```

```cpp
#include <hip/hip_runtime.h>
#include <stdint.h>

typedef unsigned u32;
typedef unsigned short u16;
typedef unsigned long long u64;
typedef u32 u32x4 __attribute__((ext_vector_type(4)));
typedef u32 u32x2 __attribute__((ext_vector_type(2)));
typedef float f32x2 __attribute__((ext_vector_type(2)));
typedef float f32x4 __attribute__((ext_vector_type(4)));
typedef float f32x16 __attribute__((ext_vector_type(16)));
typedef __bf16 bf16x2 __attribute__((ext_vector_type(2)));
typedef __bf16 bf16x8 __attribute__((ext_vector_type(8)));
typedef short s16x4 __attribute__((ext_vector_type(4)));
typedef __amdgpu_buffer_rsrc_t rsrc_t;
#define LAS __attribute__((address_space(3)))
#define DI __device__ __forceinline__

#define DLY_F1 6
#define DLY_SC 9
#define XWIN 12
#define XGAP 1
#define MAP48 1
#define H1SAME 0
#define Y_WAIT 45
#define ST_WAIT 34
#define DLY_O 14
#define DLY_DS 10
#define DLY_X 14
#define CX 1
#define CL 2
#define WO_W2 0
#define SC_WAIT 40
#define O_DMA 0
#define XFIX 1
#define CODE_PF 2
#define XFIX_N 16
#define PF_ORDER 0
#define BS_AT 20
#define DLY_P2 5
#define TS_PACE 0

constexpr int S = 2048, D = 512;
constexpr u32 MB2 = 2u << 20;
constexpr u32 WS_WQ = 0, WS_WO = 0x180000, WS_KVP = MB2, WS_DS = 2 * MB2, DS_BUF = 64 * 16 * 256, WS_YP = 3 * MB2, WS_XF = 4 * MB2,
              WS_OF = 6 * MB2, WS_SP = 8 * MB2, SP_UNIT = 2560, SP_WAVE = 640,
              WS_STAMP = 9 * MB2, WS_CTRL = WS_STAMP + 0x100000, WS_E = WS_CTRL, WS_F1 = WS_CTRL + 4096, WS_FW = WS_F1 + 2048 * 128, WS_F3 = WS_FW + 1024 * 128,
              WS_ST0 = WS_F3 + 4 * 4096, WS_CS = WS_ST0 + 1024 * 128, WS_TS = WS_CS + 1024 * 128, WS_BYTES = 10 * MB2;
static_assert(WS_TS + 512 * 128 <= WS_BYTES, "ws");
constexpr u32 OOB = 0x80000000u;
constexpr u32 L_W = 0, L_R2 = 3 * 33 * 1024;
constexpr u32 L_HC = L_R2, HC_COPY = 4160, HC_CH = 4 * HC_COPY, L_UR = L_HC + 2 * HC_CH, UR_CH = 79 * 96, L_YC = L_UR + 2 * UR_CH, YC_CH = 64 * 72;
constexpr u32 L_PK = 0  , L_PV = 72 * 128, L_PH = L_PV + 64 * 128, L_SA = L_PH + 1024;
constexpr u32 L_IMG = L_R2, IMG_RS = 224, L_O = L_R2, L_KVT = L_R2 + 32768, KVT_RS = 80, L_Z = L_KVT + 32 * KVT_RS, L_ZERO = L_Z + 128, L_LM = L_ZERO + 64, L_LR = L_LM + 128 * 16, L_OK = L_LR + 128 * 8, L_RL = L_OK + 16;
constexpr u32 LDS_BYTES = 163840;
static_assert(L_YC + 2 * YC_CH <= LDS_BYTES && L_ZERO + 64 <= LDS_BYTES, "lds");
constexpr int SC1 = 16;
constexpr u32 SENT = 0xFFFFFFFFu;
constexpr u32 SPIN_CAP = 1u << 15;

DI u32 pk(float a, float b) { return __builtin_bit_cast(u32, __builtin_convertvector(f32x2{a, b}, bf16x2)); }
DI float lo(u32 x) { return __builtin_bit_cast(float, x << 16); }
DI float hi(u32 x) { return __builtin_bit_cast(float, x & 0xFFFF0000u); }
DI float rb(float x) { return lo(pk(x, x)); }
DI float dot2(u32 a, u32 b, float c) { return __builtin_amdgcn_fdot2_f32_bf16(__builtin_bit_cast(bf16x2, a), __builtin_bit_cast(bf16x2, b), c, false); }
DI f32x2 pfma(f32x2 a, f32x2 b, f32x2 c) { return __builtin_elementwise_fma(a, b, c); }
DI f32x2 rb2(f32x2 x) { return pfma(x, f32x2{-65536.f, -65536.f}, x * 65537.f); }
DI f32x2 up2(u32 x) { return f32x2{lo(x), hi(x)}; }
DI u32 pk2(f32x2 x) { return pk(x.x, x.y); }
DI f32x16 mfma32(u32x4 a, u32x4 b, f32x16 c) { return __builtin_amdgcn_mfma_f32_32x32x16_bf16(__builtin_bit_cast(bf16x8, a), __builtin_bit_cast(bf16x8, b), c, 0, 0, 0); }
DI f32x4 mfma16(u32x4 a, u32x4 b, f32x4 c) { return __builtin_amdgcn_mfma_f32_16x16x32_bf16(__builtin_bit_cast(bf16x8, a), __builtin_bit_cast(bf16x8, b), c, 0, 0, 0); }
DI rsrc_t mkrs(const void* p, u32 bytes) { return __builtin_amdgcn_make_buffer_rsrc((void*)p, 0, bytes, 0x00020000); }
template <int AUX> DI u32x4 ld4(rsrc_t r, u32 v, u32 s) { return __builtin_amdgcn_raw_buffer_load_b128(r, v, s, AUX); }
template <int AUX> DI u32 ld1(rsrc_t r, u32 v, u32 s) { return __builtin_amdgcn_raw_buffer_load_b32(r, v, s, AUX); }
template <int AUX> DI void st4(u32x4 x, rsrc_t r, u32 v, u32 s) { __builtin_amdgcn_raw_buffer_store_b128(x, r, v + s, 0, AUX); }
template <int AUX> DI void st2(u32x2 x, rsrc_t r, u32 v, u32 s) { __builtin_amdgcn_raw_buffer_store_b64(x, r, v, s, AUX); }
template <int AUX> DI void st1(u32 x, rsrc_t r, u32 v, u32 s) { __builtin_amdgcn_raw_buffer_store_b32(x, r, v, s, AUX); }
DI void dma(rsrc_t r, u32 v, u32 ldsaddr) {
    asm volatile("s_mov_b32 m0, %0\n\ts_nop 0\n\tbuffer_load_dwordx4 %1, %2, 0 offen lds" ::"s"(ldsaddr), "v"(v), "s"(r) : "memory");
}
template <typename T> DI T lds_ld(u32 a) { return *(LAS T*)(size_t)a; }
template <typename T> DI void lds_st(u32 a, T v) { *(LAS T*)(size_t)a = v; }
DI u32x2 lds_tr(u32 a) { return __builtin_bit_cast(u32x2, __builtin_amdgcn_ds_read_tr16_b64_v4i16((LAS s16x4*)(size_t)a)); }
DI void bar() { __builtin_amdgcn_sched_barrier(0); asm volatile("s_waitcnt lgkmcnt(0)\n\ts_barrier" ::: "memory"); __builtin_amdgcn_sched_barrier(0); }
DI void drain() { asm volatile("s_waitcnt vmcnt(0)" ::: "memory"); }
DI void fence() { asm volatile("" ::: "memory"); }
DI bool spent(u32& sp) { u32 one; asm volatile("s_mov_b32 %0, 1" : "=s"(one)); sp += one; return sp > SPIN_CAP; }
template <int N> DI void delay() { if (N > 0) __builtin_amdgcn_s_sleep(N); }
DI u32 nos(u32 x) { return x < 0xFFFFFFFEu ? x : 0xFFFFFFFEu; }
DI u32x4 nos4(u32x4 x) { return u32x4{nos(x.x), nos(x.y), nos(x.z), nos(x.w)}; }
DI u32 max4(u32 m, u32x4 v) { return max(max(max(max(m, v.x), v.y), v.z), v.w); }
DI float fasu(u32 x) { return __builtin_bit_cast(float, x); }
DI u32 uasf(float x) { return __builtin_bit_cast(u32, x); }
DI float halfsum(float x) { auto r = __builtin_amdgcn_permlane32_swap(uasf(x), uasf(x), false, false); return fasu(r[0]) + fasu(r[1]); }
DI float rowpairsum(float x) { auto r = __builtin_amdgcn_permlane16_swap(uasf(x), uasf(x), false, false); return fasu(r[0]) + fasu(r[1]); }
template <int CTRL> DI float dppadd(float x) { return x + fasu(__builtin_amdgcn_update_dpp(0u, uasf(x), CTRL, 0xf, 0xf, true)); }
DI u32x4 t1(u32x4 a) {
    auto r02 = __builtin_amdgcn_permlane16_swap(a.x, a.z, false, false);
    auto r13 = __builtin_amdgcn_permlane16_swap(a.y, a.w, false, false);
    u32 a0 = r02[0], a2 = r02[1], a1 = r13[0], a3 = r13[1];
    u32x4 o;
    o.x = __builtin_amdgcn_update_dpp(a0, a1, 0x128, 0xf, 0xc, false);
    o.y = __builtin_amdgcn_update_dpp(a1, a0, 0x128, 0xf, 0x3, false);
    o.z = __builtin_amdgcn_update_dpp(a2, a3, 0x128, 0xf, 0xc, false);
    o.w = __builtin_amdgcn_update_dpp(a3, a2, 0x128, 0xf, 0x3, false);
    return o;
}
#define SB() __builtin_amdgcn_sched_barrier(0)
DI void lnstat(float sum, float sq, float& m, float& s) {
    float mean = sum * (1.f / D);
    float var = fmaxf(sq * (1.f / D) - mean * mean, 0.f);
    m = rb(mean);
    s = rb(__builtin_amdgcn_sqrtf(rb(rb(var) + 1e-5f)));
}
constexpr u32 ONE_LO = 0x00003F80u, ONE_HI = 0x3F800000u, ONE2 = 0x3F803F80u;

DI float max3(float a, float b, float c) { float r; asm("v_max3_f32 %0, %1, %2, %3" : "=v"(r) : "v"(a), "v"(b), "v"(c)); return r; }
DI float min3(float a, float b, float c) { float r; asm("v_min3_f32 %0, %1, %2, %3" : "=v"(r) : "v"(a), "v"(b), "v"(c)); return r; }
DI float max16(const f32x16& p) { return max3(max3(max3(p[0], p[1], p[2]), max3(p[3], p[4], p[5]), max3(p[6], p[7], p[8])), max3(max3(p[9], p[10], p[11]), max3(p[12], p[13], p[14]), p[15]), p[15]); }
DI float min16(const f32x16& p) { return min3(min3(min3(p[0], p[1], p[2]), min3(p[3], p[4], p[5]), min3(p[6], p[7], p[8])), min3(min3(p[9], p[10], p[11]), min3(p[12], p[13], p[14]), p[15]), p[15]); }
DI void softmax16(const f32x16& p, float c, float a, u32x4& o0, u32x4& o1) {
    f32x2 e[8];
    const f32x2 c2 = {c, c}, a2 = {a, a};
#pragma unroll
    for (int i = 0; i < 8; ++i) {
        const f32x2 t = pfma(f32x2{p[2 * i], p[2 * i + 1]}, c2, a2);
        e[i] = f32x2{__builtin_amdgcn_exp2f(t.x), __builtin_amdgcn_exp2f(t.y)};
    }

    const f32x2 s = ((e[0] + e[1]) + (e[2] + e[3])) + ((e[4] + e[5]) + (e[6] + e[7]));
    const float r = __builtin_amdgcn_rcpf(s.x + s.y);
#pragma unroll
    for (int i = 0; i < 4; ++i) { o0[i] = pk2(e[i] * r); o1[i] = pk2(e[4 + i] * r); }
}
DI u32x4 pk8(const f32x16& a, int s, float r) {
    return u32x4{pk2(f32x2{a[8 * s], a[8 * s + 1]} * r), pk2(f32x2{a[8 * s + 2], a[8 * s + 3]} * r), pk2(f32x2{a[8 * s + 4], a[8 * s + 5]} * r), pk2(f32x2{a[8 * s + 6], a[8 * s + 7]} * r)};
}
DI u32 now() { return (u32)__builtin_amdgcn_s_memrealtime(); }
DI void wait_until(u32 t0, int ticks) { while ((int)(now() - t0) < ticks) __builtin_amdgcn_s_sleep(1); }
DI int rowmax(int x) {
    x = max(x, __builtin_amdgcn_update_dpp(0, x, 0xB1, 0xf, 0xf, true));
    x = max(x, __builtin_amdgcn_update_dpp(0, x, 0x4E, 0xf, 0xf, true));
    x = max(x, __builtin_amdgcn_update_dpp(0, x, 0x124, 0xf, 0xf, true));
    return max(x, __builtin_amdgcn_update_dpp(0, x, 0x128, 0xf, 0xf, true));
}
#define RARE(x) __builtin_expect(!!(x), 0)
DI bool again(bool bad, u32& sp) { if (RARE(bad)) { __builtin_amdgcn_s_sleep(2); return !spent(sp); } return false; }

#define STAMP(i)
#define COUNT(i, v)
#define DUMP()

DI u32 wait_flags(rsrc_t r, u32 v, u32 tag) {
    u32 sp = 0;
    do { fence(); } while (again(__any(ld1<SC1>(r, v, 0) != tag), sp));
    fence();
    return sp;
}
DI void dma8k(rsrc_t r, u32 v, u32 so, u32 la) {
    asm volatile("s_nop 4\n\ts_mov_b32 m0, %0\n\ts_nop 0\n\t"
                 "buffer_load_dwordx4 %2, %3, %4 offen sc1 lds\n\tbuffer_load_dwordx4 %2, %3, %4 offen offset:1024 sc1 lds\n\t"
                 "buffer_load_dwordx4 %2, %3, %4 offen offset:2048 sc1 lds\n\tbuffer_load_dwordx4 %2, %3, %4 offen offset:3072 sc1 lds\n\t"
                 "s_mov_b32 m0, %1\n\ts_nop 0\n\t"
                 "buffer_load_dwordx4 %2, %3, %5 offen sc1 lds\n\tbuffer_load_dwordx4 %2, %3, %5 offen offset:1024 sc1 lds\n\t"
                 "buffer_load_dwordx4 %2, %3, %5 offen offset:2048 sc1 lds\n\tbuffer_load_dwordx4 %2, %3, %5 offen offset:3072 sc1 lds"
                 ::"s"(la), "s"(la + 4096), "v"(v), "s"(r), "s"(so), "s"(so + 4096) : "memory");
}
template <int IMM> DI void xfix(u32x4& x, u32x4& y, rsrc_t r, u32 v, u32 so, u32& budget) {
    u32 t;
    asm volatile("v_max_u32 %3, %4, %5\n\tv_cmp_eq_u32 vcc, -1, %3\n\ts_cbranch_vccnz 2f\n1:\n"
                 ".subsection 1\n2:\n\ts_cmp_eq_u32 %2, 0\n\ts_cbranch_scc1 1b\n\ts_sub_u32 %2, %2, 1\n\ts_sleep 2\n\t"
                 "buffer_load_dword %3, %6, %7, %8 offen offset:%c9+1024 sc1\n\tbuffer_load_dwordx4 %0, %6, %7, %8 offen offset:%c9 sc1\n\t"
                 "buffer_load_dwordx4 %1, %6, %7, %8 offen offset:%c9+1024 sc1\n\ts_waitcnt vmcnt(0)\n\t"
                 "v_cmp_eq_u32 vcc, -1, %3\n\ts_cbranch_vccnz 2b\n\ts_branch 1b\n.subsection 0"
                 : "+v"(x), "+v"(y), "+s"(budget), "=&v"(t) : "v"(x.x), "v"(y.x), "v"(v), "s"(r), "s"(so), "i"(IMM) : "vcc", "scc", "memory");
}
DI u32 o_gather(rsrc_t r, u32 v, u32 so, u32 la) {
    u32 sp = 0; bool bad;
    do {
        asm volatile("" : "+v"(v) :: "memory");
        u32x4 o[4]; u32 m = 0;
#pragma unroll
        for (int i = 0; i < 4; ++i) o[i] = ld4<SC1>(r, v + i * 1024, so);
        SB();
#pragma unroll
        for (int i = 0; i < 4; ++i) { lds_st<u32x4>(la + i * 1024, o[i]); m = max4(m, o[i]); SB(); }
        bad = __any(m == SENT);
    } while (again(bad, sp));
    return sp;
}
DI void stats_publish(const u32x4 (&x)[2], rsrc_t r, u32 v, u32 off) {
    float s0 = 0.f, s1 = 0.f, q0 = 0.f, q1 = 0.f;
#pragma unroll
    for (int i = 0; i < 4; ++i) { s0 = dot2(x[0][i], ONE2, s0); q0 = dot2(x[0][i], x[0][i], q0); s1 = dot2(x[1][i], ONE2, s1); q1 = dot2(x[1][i], x[1][i], q1); }
    auto p = __builtin_amdgcn_permlane32_swap(uasf(s0 + s1), uasf(q0 + q1), false, false);
    st1<SC1>(nos(uasf(fasu(p[0]) + fasu(p[1]))), r, v, off);
}
DI void stats_issue(u32x4 (&ds)[4], rsrc_t r, u32 v, u32 off) {
#pragma unroll
    for (int i = 0; i < 4; ++i) ds[i] = ld4<SC1>(r, v + i * 1024, off);
}
DI float stats_sum(const u32x4 (&ds)[4]) {
    const f32x4 a = (__builtin_bit_cast(f32x4, ds[0]) + __builtin_bit_cast(f32x4, ds[1])) + (__builtin_bit_cast(f32x4, ds[2]) + __builtin_bit_cast(f32x4, ds[3]));
    auto p0 = __builtin_amdgcn_permlane32_swap(uasf(a[0]), uasf(a[2]), false, false);
    auto p1 = __builtin_amdgcn_permlane32_swap(uasf(a[1]), uasf(a[3]), false, false);
    auto p2 = __builtin_amdgcn_permlane16_swap(uasf(fasu(p0[0]) + fasu(p0[1])), uasf(fasu(p1[0]) + fasu(p1[1])), false, false);
    return fasu(p2[0]) + fasu(p2[1]);
}
DI u32 stats_check(u32x4 (&ds)[4], rsrc_t r, u32 v, u32 off, float& t) {
    u32 sp = 0;
    t = stats_sum(ds);
    if (RARE(__any(t != t))) {
        while (again(__any(max4(max4(max4(max4(0, ds[0]), ds[1]), ds[2]), ds[3]) == SENT), sp)) { asm volatile("" : "+v"(v) :: "memory"); stats_issue(ds, r, v, off); }
        t = stats_sum(ds);
        __builtin_amdgcn_s_waitcnt(0x0F70);
    }
    return sp;
}
DI void stats_finish(float t, float& mean, float& rs) {
    auto p = __builtin_amdgcn_permlane32_swap(uasf(t), uasf(t), false, false);
    float sd; lnstat(fasu(p[0]), fasu(p[1]), mean, sd);
    rs = __builtin_amdgcn_rcpf(sd) * 1.0000003f;
}

typedef u64 u64x8 __attribute__((ext_vector_type(8)));
typedef u64 u64x2 __attribute__((ext_vector_type(2)));
__global__ void __launch_bounds__(512, 2) h3_hedgehog_kernel(const u16*, const u16*, const u16*, const u16*, const u16*, const u16*,
                                                             const u16*, const u16*, const u16*, u16*, char*) {
    u64x8 ka; u64x2 kb; u64 kc;
    asm volatile("s_load_dwordx16 %0, %3, 0x0\n\ts_load_dwordx4 %1, %3, 0x40\n\ts_load_dwordx2 %2, %3, 0x50"
                 : "=&s"(ka), "=&s"(kb), "=&s"(kc) : "s"(__builtin_amdgcn_kernarg_segment_ptr()));
    SB();
    extern __shared__ __attribute__((aligned(16))) char smem[];
    const u32 tid = threadIdx.x, lane = tid & 63, wave = __builtin_amdgcn_readfirstlane(tid >> 6), b = blockIdx.x;
    const u32 xcd = b & 7, slot = b >> 3;
    const u32 L0 = (u32)(size_t)(LAS char*)smem;
    u32 junk, junk2;
    auto code_pf = [&]() __attribute__((always_inline)) {
        asm volatile("s_getpc_b64 s[60:61]\n.Lh3_here:\n\ts_min_u32 %0, %2, .Lh3_end-.Lh3_here\n\ts_load_dword %1, s[60:61], %0 offset:64\n\ts_load_dword %0, s[60:61], %0"
                     : "=&s"(junk), "=&s"(junk2) : "s"((PF_ORDER ? slot * 8 + wave : wave * 32 + slot) * 128) : "scc", "s60", "s61", "memory");
    };
    if (CODE_PF == 1) code_pf();
    const u32 tt = H1SAME ? slot : xcd * 4 + (slot >> 3), cg = H1SAME ? xcd : slot & 7;
    const u32 grp = MAP48 ? (xcd >> 1) * 4 + (slot & 3) : xcd * 2 + (slot & 1);
    const u32 rank = MAP48 ? (xcd & 1) * 8 + (slot >> 2) : slot >> 1;
    const u32 lane16 = lane * 16;
    u32x4 wsk;
    const u16 *gq, *gk, *gv, *gsk, *gh, *gD, *gW, *gphi, *gwo; u16* gout; char* ws;
    {
        const u32 row = tid >> 3, c8 = tid & 7;
        const u32 g0 = (tt * 64 + row) * 1024 + cg * 128;
        u32 ok = g0 + ((c8 ^ (((row + 8) >> 2) & 7)) << 4), ov = g0 + ((c8 ^ ((row >> 2) & 7)) << 4);
        u32 oh = ((ov - 8192) | (tt == 0 ? OOB : 0u)) & 0xBFFFFFFFu, op = rank * 1024 + lane16, os = cg * 512 + (tid & ~15u);
        asm volatile("" : "+v"(ok), "+v"(ov), "+v"(oh), "+v"(op), "+v"(os));
        SB();
        asm volatile("s_waitcnt lgkmcnt(0)" : "+s"(ka), "+s"(kb), "+s"(kc));
        gq = (const u16*)ka[0]; gk = (const u16*)ka[1]; gv = (const u16*)ka[2]; gsk = (const u16*)ka[3]; gh = (const u16*)ka[4]; gD = (const u16*)ka[5];
        gW = (const u16*)ka[6]; gphi = (const u16*)ka[7]; gwo = (const u16*)kb[0]; gout = (u16*)kb[1]; ws = (char*)kc;
        if (CODE_PF == 2) code_pf();
        const rsrc_t rk = mkrs(gk, S * D * 2), rv = mkrs(gv, S * D * 2);
        asm volatile("buffer_load_dwordx4 %0, %1, %2, 0 offen" : "=&v"(wsk) : "v"(os), "s"(mkrs(gsk, D * 4 * 2)) : "memory");
        dma(rk, ok, L0 + L_PK + (wave + 1) * 1024);
        dma(rv, ov, L0 + L_PV + wave * 1024);
        if (wave == 0) dma(rk, oh, L0 + L_PK);
        if (wave == 1) dma(mkrs(gphi, 32 * 256 * 2), op, L0 + L_PH);
    }
    asm volatile("s_barrier" ::: "memory");
    const rsrc_t rw = mkrs(ws, WS_BYTES);
    const u32 pair = xcd * 32 + slot;
    const u32 rho = lane & 31, hh = lane >> 5, g16 = lane >> 4, l16 = lane & 15;
    const u32 w4 = wave & 3, Td = grp * 4 + (rank >> 2), tile = (rank & 3) * 4 + w4;
    const u32x4 sent4 = {SENT, SENT, SENT, SENT};
    const u32 srow = grp * 128 + rank * 8 + w4 * 2 + hh;
    u32x4 sa[2], xrow[2], hA; u32x2 hB;
    {
        const bool isW = wave < 3, isC = wave < 4;
        const rsrc_t r = mkrs(isW ? gW : isC ? gwo : gq, isW ? D * 3 * D * 2 : isC ? D * D * 2 : S * D * 2);
        const u32 rs_ = isW ? 3072 : 1024;
        const u32 sb = (isC ? grp : Td) * 32 * rs_ + (isW ? wave * 1024 : 0) + (isC ? rank : tile) * 64;
        const u32 o = __umul24(lane >> 2, rs_) + sb + (((lane ^ (lane >> 3)) & 3) << 4);
        dma(r, o, L0 + L_SA + wave * 2048);
        dma(r, o + 16 * rs_, L0 + L_SA + wave * 2048 + 1024);
        const rsrc_t rq = mkrs(gq, isC ? S * D * 2 : 0);
#pragma unroll
        for (int i = 0; i < 2; ++i) xrow[i] = ld4<0>(rq, srow * 1024 + i * 512 + rho * 16, 0);
        const rsrc_t rh = mkrs(gh + (size_t)(pair * 2 + (wave >> 2)) * S, S * 2);
        hA = ld4<0>(rh, (tid & 255) * 16, 0);
        hB = __builtin_amdgcn_raw_buffer_load_b64(rh, (tid & 255) * 16 + 16, 0, 0);
    }
    fence();
    const u32 tagv = ld1<0>(rw, 0, WS_E);
    fence();
    if (wave >= 4) __builtin_amdgcn_s_setprio(3);
    u32 tag, ofl;
    {
        const u32 pp = tid >> 4, tq = tid & 15;
        const u32 cw = (pp & 3) * 4, pch = pp >> 2;
        u32 a1 = L0 + L_PK + (4 * tq + 4) * 128 + ((pch ^ ((tq + 1) & 7)) << 4) + cw;
        u32 a2 = L0 + L_PK + (4 * tq + 8) * 128 + ((pch ^ ((tq + 2) & 7)) << 4) + cw;
        u32 a3 = L0 + L_PV + (4 * tq) * 128 + ((pch ^ (tq & 7)) << 4) + cw;
        u32 okv = WS_KVP + (cg * 32 + pp) * 8192 + (tt * 64 + 4 * tq) * 4;
        const u32 m8 = lane < 8 ? 0u : OOB;
        ofl = (WS_F1 + ((cg * 8 + wave) * 32 + tt) * 128 + lane16) | m8;
        u32 oc1 = WS_YP + pair * 8192 + tid * 16, oc2 = WS_XF + (Td * 32 + 2 * tile + (wave >> 2)) * 1024 + lane16, oc3 = (WS_DS + (Td * 16 + tile) * 256 + (wave >> 2) * 128 + lane16) | m8;
        asm volatile("" : "+v"(a1), "+v"(a2), "+v"(a3), "+v"(okv), "+v"(ofl), "+v"(oc1), "+v"(oc2), "+v"(oc3));
        asm volatile("s_waitcnt vmcnt(9)" : "+v"(wsk) :: "memory");
        f32x2 w1[4], w2[4];
#pragma unroll
        for (int j = 0; j < 4; ++j) {
            const f32x2 w = (j & 1) ? f32x2{hi(wsk[j >> 1]), hi(wsk[2 + (j >> 1)])} : f32x2{lo(wsk[j >> 1]), lo(wsk[2 + (j >> 1)])};
            w1[j] = w * 65537.f; w2[j] = w * -65536.f;
            asm volatile("" : "+v"(w1[j]), "+v"(w2[j]));
        }
        u32x4 s4 = sent4;
        asm volatile("" : "+v"(s4));
        SB();
        asm volatile("s_waitcnt vmcnt(7)" ::: "memory");
        STAMP(1);
        bar();
        if (CODE_PF) asm volatile("" ::"s"(junk), "s"(junk2));
        u32 kk[7], vv[4];
#pragma unroll
        for (int i = 0; i < 3; ++i) kk[i] = lds_ld<u32>(a1 + (i + 1) * 128);
#pragma unroll
        for (int i = 0; i < 4; ++i) kk[3 + i] = lds_ld<u32>(a2 + i * 128);
#pragma unroll
        for (int i = 0; i < 4; ++i) vv[i] = lds_ld<u32>(a3 + i * 128);
        SB();
        st4<SC1>(s4, rw, oc1, 0);
        if (CX == 1) { st4<SC1>(s4, rw, oc2, 0); st4<SC1>(s4, rw, oc3, 0); }
        SB();
        f32x2 kf[7], sm[4];
#pragma unroll
        for (int i = 0; i < 7; ++i) kf[i] = up2(kk[i]);
#pragma unroll
        for (int i = 0; i < 4; ++i) sm[i] = pfma(kf[i], w2[3], kf[i] * w1[3]);
#pragma unroll
        for (int j = 2; j >= 0; --j)
#pragma unroll
            for (int i = 0; i < 4; ++i) sm[i] = rb2(sm[i] + pfma(kf[i + 3 - j], w2[j], kf[i + 3 - j] * w1[j]));
        u32x4 kv;
#pragma unroll
        for (int i = 0; i < 4; ++i) kv[i] = pk2(sm[i] * up2(vv[i]));
        st4<SC1>(kv, rw, okv, 0);
        SB();
        STAMP(2);
        __builtin_amdgcn_s_setprio(0);
        tag = __builtin_amdgcn_readfirstlane(tagv) + 1u;
        asm volatile("" ::"v"(xrow[0]), "v"(xrow[1]), "v"(hA), "v"(hB) : "memory");
        const u32 row = wave < 3 ? (rho & 0x13) | ((rho & 4) << 1) | ((rho & 8) >> 1) : wave == 3 ? (rho & 3) | ((rho & 4) << 2) | ((rho & 24) >> 1) : 4 * (rho & 7) + (rho >> 3);
        const u32 a = L0 + L_SA + wave * 2048 + row * 64 + (((hh ^ (row >> 1)) & 3) << 4);
        sa[0] = lds_ld<u32x4>(a); sa[1] = lds_ld<u32x4>(a ^ 32);
        if (CX == 2) { st4<SC1>(s4, rw, oc2, 0); st4<SC1>(s4, rw, oc3, 0); }
    }
    SB();
    const u32x4 tag4 = {tag, tag, tag, tag};
    if (wave < 4) {
        {
            u32x4 bf; u32 sel;
            if (wave < 2) {
                sel = (rho >> 2) & 1;
                const u32 a = L0 + L_PH + (lane & 1) * 512 + (hh * 8 + (l16 >> 2)) * 32 + ((g16 & 1) * 2 + ((lane >> 1) & 1)) * 8;
                const u32x2 p0 = lds_tr(a), p1 = lds_tr(a + 128);
                bf = u32x4{p0.x, p0.y, p1.x, p1.y};
            } else {
                const u32 n = wave < 3 ? (((rho >> 2) & 1) * 16 + (rho & 3) + 4 * (rho >> 3)) : ((rho & 0x13) | ((rho & 4) << 1) | ((rho & 8) >> 1));
                sel = n >> 4;
                const u32 e = (n & 15) - hh * 8;
                const u32 one = (e & 1) ? ONE_HI : ONE_LO;
                bf = u32x4{(e >> 1) == 0 ? one : 0u, (e >> 1) == 1 ? one : 0u, (e >> 1) == 2 ? one : 0u, (e >> 1) == 3 ? one : 0u};
            }
            const u32x4 z4 = {0, 0, 0, 0};
            f32x16 acc = {};
            acc = mfma32(sa[0], sel == 0 ? bf : z4, acc);
            acc = mfma32(sa[1], sel == 1 ? bf : z4, acc);
            const u32 o = (wave < 3 ? WS_WQ + ((rank * 3 + wave) * 32 + grp * 2) * 1024 : WS_WO + (rank * 32 + grp * 2) * 1024);
            float cs = 0.f;
#pragma unroll
            for (int s = 0; s < 2; ++s) {
                const u32x4 w = {pk(acc[8 * s], acc[8 * s + 1]), pk(acc[8 * s + 2], acc[8 * s + 3]), pk(acc[8 * s + 4], acc[8 * s + 5]), pk(acc[8 * s + 6], acc[8 * s + 7])};
                st4<SC1>(w, rw, lane16 + s * 1024, o);
#pragma unroll
                for (int i = 0; i < 4; ++i) cs = dot2(w[i], ONE2, cs);
            }
            st1<SC1>(uasf(halfsum(cs)), rw, lane < 32 ? WS_CS + ((rank * 4 + wave) * 16 + grp) * 128 + lane * 4 : OOB, 0);
        }
        STAMP(14);
        if (CX == 2) asm volatile("s_waitcnt vmcnt(5)" ::: "memory"); else asm volatile("s_waitcnt vmcnt(3)" ::: "memory");
        STAMP(32);
        st4<SC1>(tag4, rw, ofl, 0);
        SB();
        {
            float sum = 0.f, sq = 0.f;
#pragma unroll
            for (int j = 0; j < 8; ++j) { sum = dot2(xrow[j >> 2][j & 3], ONE2, sum); sq = dot2(xrow[j >> 2][j & 3], xrow[j >> 2][j & 3], sq); }
            sum = rowpairsum(dppadd<0x128>(dppadd<0x124>(dppadd<0x4E>(dppadd<0xB1>(sum)))));
            sq = rowpairsum(dppadd<0x128>(dppadd<0x124>(dppadd<0x4E>(dppadd<0xB1>(sq)))));
            float m, s; lnstat(sum, sq, m, s);
            st2<SC1>(u32x2{pk(m, s), tag}, rw, rho < 8 ? srow * 64 + rho * 8 : OOB, WS_ST0);
        }
        asm volatile("s_waitcnt vmcnt(2)" ::: "memory");
        st4<SC1>(tag4, rw, wave < 3 && lane < 8 ? WS_FW + ((rank * 16 + grp) * 4 + wave) * 128 + lane16 : OOB, 0);
        STAMP(3);
        if (tid < 180) { const u32 c = tid >= 90; lds_st<u32x4>(L0 + L_UR + c * UR_CH + 64 * 96 + (tid - c * 90) * 16, u32x4{0, 0, 0, 0}); }
    } else {
        STAMP(37);
        if (CX == 2) asm volatile("s_waitcnt vmcnt(2)" ::: "memory"); else drain();
        st4<SC1>(tag4, rw, ofl, 0);
        STAMP(36);
    }
    {
        const u32 hq = tid & 255;
        u32x4 a = hA; u32x2 c = hB; u32 p = L0 + L_HC + (tid >> 8) * HC_CH + (hq + 4) * 16;
#pragma unroll 1
        for (int it = 0; it < 2; ++it) {
            const u32 d[6] = {a.x, a.y, a.z, a.w, c.x, c.y};
            u32 al[5];
#pragma unroll
            for (int i = 0; i < 5; ++i) al[i] = __builtin_amdgcn_alignbit(d[i + 1], d[i], 16);
            lds_st<u32x4>(p, u32x4{al[0], al[1], al[2], al[3]});
            lds_st<u32x4>(p + HC_COPY, u32x4{d[1], d[2], d[3], d[4]});
            lds_st<u32x4>(p + 2 * HC_COPY, u32x4{al[1], al[2], al[3], al[4]});
            lds_st<u32x4>(p + 3 * HC_COPY, u32x4{d[2], d[3], d[4], d[5]});
            if (hq >= 4) break;
            c = hq == 0 ? u32x2{a.x, a.y} : u32x2{0, 0};
            a = u32x4{0, 0, 0, 0};
            p -= (2 * hq + 1) * 16;
        }
    }
    if (wave < 4) {
        u32 tid_c = tid;
        asm volatile("" : "+v"(tid_c));
        const u32 lane = tid_c & 63, lane16 = lane * 16, rho = lane & 31, hh = lane >> 5, g16 = lane >> 4, l16 = lane & 15;
        const u32 ch = wave >> 1, it = wave & 1;
        u32 aA = L0 + L_HC + ch * HC_CH + (l16 & 3) * HC_COPY + 2 * (16 * it + 8 * g16 + (l16 & 12));
        u32 aB = L0 + L_UR + ch * UR_CH + (15 - l16) * 96 + 16 * g16;
        u32 aY = L0 + L_YC + ch * YC_CH + l16 * 72 + (16 * it + 4 * g16) * 2;
        u32 f3o = wave == 0 ? WS_F3 + (b & 3) * 4096 + lane * 64 : OOB;
        f32x4 acc[4] = {};
        asm volatile("" : "+v"(aA), "+v"(aB), "+v"(aY), "+v"(f3o), "+v"(acc[0]), "+v"(acc[1]), "+v"(acc[2]), "+v"(acc[3]));
        STAMP(13);
        bar();
        STAMP(4);
        {
            asm volatile(
                ".macro H3MM d, a, u\n\tv_mfma_f32_16x16x32_bf16 \\d, v[\\a:\\a+3], v[\\u:\\u+3], \\d\n.endm\n"
                ".macro H3RA r, i, n, o\n\tds_read_b64 v[\\r+4*\\i+2*\\o:\\r+4*\\i+2*\\o+1], %4 offset:1024*\\i+64*\\n+8*\\o\n.endm\n"
                ".macro H3RU r, i, n\n\tds_read_b128 v[\\r+16+4*\\i:\\r+16+4*\\i+3], %5 offset:1536*(3-\\i)+96*\\n\n.endm\n"
                ".macro H3STEP x, y, n\n"
                "s_waitcnt lgkmcnt(6)\n"
                "H3MM %0, \\x, \\x+16\n H3RA \\y, 0, \\n, 0\n H3RA \\y, 0, \\n, 1\n"
                "H3MM %1, \\x, \\x+20\n H3RU \\y, 0, \\n\n"
                "H3MM %2, \\x, \\x+24\n H3RU \\y, 1, \\n\n"
                "H3MM %3, \\x, \\x+28\n H3RU \\y, 2, \\n\n"
                "s_waitcnt lgkmcnt(9)\n"
                "H3MM %1, \\x+4, \\x+16\n H3RU \\y, 3, \\n\n"
                "H3MM %2, \\x+4, \\x+20\n H3RA \\y, 1, \\n, 0\n H3RA \\y, 1, \\n, 1\n"
                "H3MM %3, \\x+4, \\x+24\n H3RA \\y, 2, \\n, 0\n"
                "s_waitcnt lgkmcnt(9)\n"
                "H3MM %2, \\x+8, \\x+16\n H3RA \\y, 2, \\n, 1\n"
                "H3MM %3, \\x+8, \\x+20\n H3RA \\y, 3, \\n, 0\n"
                "H3MM %3, \\x+12, \\x+16\n H3RA \\y, 3, \\n, 1\n"
                ".endm\n"
                "H3RA 64, 0, 0, 0\n H3RA 64, 0, 0, 1\n H3RU 64, 0, 0\n H3RU 64, 1, 0\n H3RU 64, 2, 0\n H3RU 64, 3, 0\n"
                "H3RA 64, 1, 0, 0\n H3RA 64, 1, 0, 1\n H3RA 64, 2, 0, 0\n H3RA 64, 2, 0, 1\n H3RA 64, 3, 0, 0\n H3RA 64, 3, 0, 1\n"
                "s_mov_b32 s60, 4\n"
                ".Lh3_conv:\n"
                "H3STEP 64, 96, 1\n"
                "H3STEP 96, 64, 2\n"
                "H3STEP 64, 96, 3\n"
                "s_sub_u32 s60, s60, 1\n"
                "H3STEP 96, 64, 4\n"
                "v_add_u32 %4, 0x100, %4\n v_add_u32 %5, 0x180, %5\n"
                "s_cmp_lg_u32 s60, 0\n"
                "s_cbranch_scc1 .Lh3_conv\n"
                "s_waitcnt lgkmcnt(0)\n s_nop 7\n"
                ".purgem H3MM\n.purgem H3RA\n.purgem H3RU\n.purgem H3STEP\n"
                : "+v"(acc[0]), "+v"(acc[1]), "+v"(acc[2]), "+v"(acc[3]), "+v"(aA), "+v"(aB)
                :
                : "memory", "scc", "s60", "v64", "v65", "v66", "v67", "v68", "v69", "v70", "v71", "v72", "v73", "v74", "v75", "v76", "v77", "v78", "v79",
                  "v80", "v81", "v82", "v83", "v84", "v85", "v86", "v87", "v88", "v89", "v90", "v91", "v92", "v93", "v94", "v95",
                  "v96", "v97", "v98", "v99", "v100", "v101", "v102", "v103", "v104", "v105", "v106", "v107", "v108", "v109", "v110", "v111",
                  "v112", "v113", "v114", "v115", "v116", "v117", "v118", "v119", "v120", "v121", "v122", "v123", "v124", "v125", "v126", "v127");
#pragma unroll
            for (int c = 0; c < 4; ++c) lds_st<u32x2>(aY + 16 * 72 * c, u32x2{pk(acc[c][0], acc[c][1]), pk(acc[c][2], acc[c][3])});
        }
        STAMP(5);
        u32x4 f3[2];
        f3[0] = ld4<SC1>(rw, f3o, 0); f3[1] = ld4<SC1>(rw, f3o + 16, 0);
        bar();
        if (wave == 0) {
            const u32 tyc = now();
            u32 sp = 0;
            while (again(__any(((f3[0].y ^ tag) | (f3[0].w ^ tag) | (f3[1].y ^ tag) | (f3[1].w ^ tag)) != 0), sp)) {
                asm volatile("" : "+v"(f3o) :: "memory");
                f3[0] = ld4<SC1>(rw, f3o, 0); f3[1] = ld4<SC1>(rw, f3o + 16, 0);
            }
            fence();
            COUNT(27, sp);
            STAMP(23);
            const u32 own = __builtin_amdgcn_readlane((b & 2) ? ((b & 1) ? f3[1].z : f3[1].x) : ((b & 1) ? f3[0].z : f3[0].x), b >> 2);
            int lag = rowmax(max(max((int)(f3[0].x - own), (int)(f3[0].z - own)), max((int)(f3[1].x - own), (int)(f3[1].z - own))));
            lag = max(max(__builtin_amdgcn_readlane(lag, 0), __builtin_amdgcn_readlane(lag, 16)), max(__builtin_amdgcn_readlane(lag, 32), __builtin_amdgcn_readlane(lag, 48)));
            lag = min(lag, 1000) + Y_WAIT;
            COUNT(33, lag);
            wait_until(tyc, lag);
        }
        STAMP(25);
        bar();
        const u32 T = grp * 4 + wave;
        const u32 aW = L0 + L_W + lane16;
        u32 aW1 = aW + 33 * 1024, aW2 = aW + 66 * 1024;
        u32 aI = L0 + L_IMG + (wave * 32 + rho) * IMG_RS + hh * 64, aLM = L0 + L_LM + (wave * 32 + rho) * 16;
        u32 aN = ((rho >> 2) & 1) == hh ? L0 + L_KVT + (hh * 16 + (rho & 3) + 4 * (rho >> 3)) * KVT_RS : L0 + L_ZERO;
        u32 aZ = (l16 >> 2) == g16 ? L0 + L_Z + (g16 >> 1) * 64 : L0 + L_ZERO;
        u32 oo = WS_OF + (T * 32 + 2 * rank) * 1024 + lane16;
        asm volatile("" : "+v"(aW1), "+v"(aW2), "+v"(aI), "+v"(aLM), "+v"(aN), "+v"(aZ), "+v"(oo));
#pragma unroll 1
        for (int layer = 0; layer < 2; ++layer) {
            delay<2>();
            st4<SC1>(sent4, rw, oo, 0); st4<SC1>(sent4, rw, oo + 1024, 0);
            drain();
            STAMP(34);
            bar();
            STAMP(6 + 10 * layer);
            delay<DLY_X>();
            const u32 xb = WS_XF + layer * MB2 + T * 32768;
            u32 xo = lane16;
            u32x4 X[32], xm; f32x2 rs2;
            f32x16 aK, aV;
            u32 tries = 0, budget = SPIN_CAP; bool bad;
            do {
                asm volatile("" : "+v"(xo) :: "memory");
#pragma unroll
                for (int i = 0; i < XWIN; ++i) { X[i] = ld4<SC1>(rw, xo + (i & 3) * 1024, xb + (i >> 2) * 4096); if (i < XWIN - 1) delay<XGAP>(); SB(); }
                if (tries == 0) asm volatile("s_barrier" ::: "memory");
                aK = f32x16{}; aV = f32x16{};
                u32x4 wk[3], wv[3];
                constexpr int MS = BS_AT + 4;
                auto fr = [](int j) { return j < MS ? j : j == MS ? 32 : j - 1; };
#pragma unroll
                for (int j = 0; j < 2; ++j) { wk[j] = lds_ld<u32x4>(aW1 + j * 1024); wv[j] = lds_ld<u32x4>(aW2 + j * 1024); }
                SB();
                if (XFIX) xfix<0>(X[0], X[1], rw, xo, xb, budget);
                SB();
#pragma unroll
                for (int j = 0; j < 33; ++j) {
                    const int i = fr(j);
                    asm volatile("" ::"v"(wv[j % 3]));
                    SB();
                    aK = mfma32(wk[j % 3], i < 32 ? X[i & 31] : xm, aK);
                    if (j == 0) STAMP(24 + 16 * (1 - layer)); if (j == 20) STAMP(26 + 16 * (1 - layer));
                    SB();
                    if (j == BS_AT + 1) { xm = lds_ld<u32x4>(aLM); rs2 = lds_ld<f32x2>((aLM >> 1) + (L_LR - L_LM / 2) + L0 / 2); }
                    if (j < 31) wk[(j + 2) % 3] = lds_ld<u32x4>(aW1 + fr(j + 2) * 1024);
                    if (j + XWIN < 32) X[(j + XWIN) & 31] = ld4<SC1>(rw, xo + ((j + XWIN) & 3) * 1024, xb + ((j + XWIN) >> 2) * 4096);
                    SB();
                    aV = mfma32(wv[j % 3], i < 32 ? X[i & 31] : xm, aV);
                    SB();
                    if (XFIX && j < 32 && fr(j + 1) < XFIX_N && (fr(j + 1) & 1) == 0) {
                        const int n = fr(j + 1) & 31;
                        if ((n & 2) == 0) xfix<0>(X[n], X[(n + 1) & 31], rw, xo, xb + (n >> 2) * 4096, budget);
                        else xfix<2048>(X[n], X[(n + 1) & 31], rw, xo, xb + (n >> 2) * 4096, budget);
                    }
                    SB();
                    if (j == BS_AT && tries == 0) asm volatile("s_barrier" ::: "memory");
                    if (j < 31) wv[(j + 2) % 3] = lds_ld<u32x4>(aW2 + fr(j + 2) * 1024);
                    SB();
                    SB();
                }
                bad = __any(aK[0] != aK[0]);
                if (RARE(bad)) {
                    u32 m = 0;
#pragma unroll
                    for (int i = 0; i < 32; ++i) m = max4(m, X[i]);
                    bad = __any(m == SENT) && ++tries < 4096;
                }
            } while (RARE(bad));
            COUNT(28 + layer, tries * 1000 + SPIN_CAP - budget);
            STAMP(7 + 10 * layer);
            float rs = rs2.x, c = rs2.y;
            asm volatile("" : "+v"(rs), "+v"(c));
            u32x4 phi[4];
            softmax16(aK, c, -max16(aK) * c, phi[0], phi[1]);
            const float mn = min16(aK) * c;
            lds_st<u32x4>(aI, phi[0]); lds_st<u32x4>(aI + 16, phi[1]);
            SB();
            lds_st<u32x4>(aI + 128 - hh * 32, pk8(aV, 0, rs)); lds_st<u32x4>(aI + 144 - hh * 32, pk8(aV, 1, rs));
            SB();
            softmax16(aK, -c, mn, phi[2], phi[3]);
            lds_st<u32x4>(aI + 32, phi[2]); lds_st<u32x4>(aI + 48, phi[3]);
            STAMP(8 + 10 * layer);
            bar();
            delay<DLY_P2>();
            f32x16 aQ = {};
            {
                u32x4 wq[4];
#pragma unroll
                for (int i = 0; i < 3; ++i) wq[i] = lds_ld<u32x4>(aW + i * 1024);
                SB();
#pragma unroll
                for (int i = 0; i < 33; ++i) {
                    if (i + 3 < 33) wq[(i + 3) & 3] = lds_ld<u32x4>(aW + (i + 3) * 1024);
                    aQ = mfma32(wq[i & 3], i < 32 ? X[i & 31] : xm, aQ);
                    SB();
                }
            }
            asm volatile("s_nop 11" : "+v"(aQ));
            softmax16(aQ, c, -max16(aQ) * c, phi[0], phi[1]);
            softmax16(aQ, -c, min16(aQ) * c, phi[2], phi[3]);
            asm volatile("" : "+v"(phi[0]), "+v"(phi[1]), "+v"(phi[2]), "+v"(phi[3]));
            STAMP(9 + 10 * layer);
            bar();
            STAMP(10 + 10 * layer);
            {
                u32x4 zf[4], nf[4];
#pragma unroll
                for (int s = 0; s < 4; ++s) zf[s] = lds_ld<u32x4>(aZ + s * 16);
#pragma unroll
                for (int s = 0; s < 4; ++s) nf[s] = lds_ld<u32x4>(aN + s * 16);
                SB();
                f32x16 num = {}; f32x4 den = {};
#pragma unroll
                for (int s = 0; s < 4; ++s) den = mfma16(zf[s], phi[s], den);
#pragma unroll
                for (int s = 0; s < 4; ++s) num = mfma32(nf[s], phi[s], num);
                const float r = __builtin_amdgcn_rcpf(den[0] + 1e-6f);
#pragma unroll
                for (int s = 0; s < 2; ++s) st4<SC1>(nos4(pk8(num, s, r)), rw, oo + s * 1024, 0);
            }
            STAMP(11 + 10 * layer);
            if (O_DMA) {
                delay<DLY_O>();
                dma8k(rw, lane16, WS_OF + ((layer * 64 + Td) * 32 + wave * 8) * 1024, L0 + L_O + wave * 8192);
                drain();
            } else {
                bar();
                delay<DLY_O>();
                const u32 sp = o_gather(rw, lane16, WS_OF + ((layer * 64 + Td) * 32 + wave * 4) * 1024, L0 + L_O + wave * 4096 + lane16);
                COUNT(30 + layer, sp);
            }
            STAMP(12 + 10 * layer);
            bar();
            if (O_DMA) {
                u32 sp = 0; bool bad;
                do {
                    fence();
                    u32 m = 0;
#pragma unroll
                    for (int i = 0; i < 8; ++i) m = max4(m, lds_ld<u32x4>(L0 + L_O + wave * 8192 + i * 1024 + lane16));
                    bad = again(__any(m == SENT), sp);
                    if (RARE(bad)) { dma8k(rw, lane16, WS_OF + ((layer * 64 + Td) * 32 + wave * 8) * 1024, L0 + L_O + wave * 8192); drain(); }
                } while (RARE(bad));
                if (lane == 0) __hip_atomic_fetch_add((LAS u32*)(size_t)(L0 + L_OK), 1u, __ATOMIC_RELAXED, __HIP_MEMORY_SCOPE_WORKGROUP);
            }
            oo += MB2;
        }
        DUMP();
        asm volatile(".subsection 2\n.Lh3_end:\n\t.fill 64, 4, 0xBF800000\n.subsection 0");
        __builtin_amdgcn_endpgm();
    } else {
        __builtin_amdgcn_s_setprio(3);
        const u32 v = wave - 4, dt = tid - 256, T = Td;
        const u32 dD = *(const __attribute__((address_space(4))) u32*)(gD + 2 * pair);
        const u32 tok = T * 32 + 4 * (rho & 7) + (rho >> 3);
        const u32 xoff = WS_XF + (T * 32 + 2 * tile) * 1024 + lane16;
        const u32 dpos = l16 * 16 + g16 * 4, dslot = WS_DS + (T * 16 + tile) * 256;
        u32x4 xr[2] = {sa[0], sa[1]};
        const u32 so = (WS_SP + (rank * 16 + grp) * SP_UNIT + v * SP_WAVE + lane16) | (lane < 40 ? 0u : OOB);
        auto clear2 = [&]() __attribute__((always_inline)) {
            st4<SC1>(sent4, rw, so + 256 * SP_UNIT, 0);
            st4<SC1>(sent4, rw, xoff + MB2, 0); st4<SC1>(sent4, rw, xoff + MB2 + 1024, 0);
            st4<SC1>(sent4, rw, (dslot + (1 + g16) * DS_BUF + l16 * 16) | (lane < 32 ? 0u : OOB), 0);
            fence();
        };
        u32x4 R[32];
        u32 cp[6];
        f32x2 xf[8], b1[8], b2[8];
        {
            const u32 qoff = pair * 8192 + v * 2048 + lane16;
            u32x4 kv[2]; u32 au[2], ay[2];
#pragma unroll
            for (int i = 0; i < 2; ++i) {
                const u32 t0 = 4 * (v * 128 + i * 64 + lane), i0 = 2044 - t0;
                au[i] = L0 + L_UR + (i0 >> 5) * 96 + (i0 & 31) * 2; ay[i] = L0 + L_YC + (t0 >> 5) * 72 + (t0 & 31) * 2;
            }
            f32x2 d1 = up2(dD) * 65537.f, d2 = up2(dD) * -65536.f;
            u32 selLo = ONE_LO, selHi = ONE_HI, oy = WS_YP + qoff, ok = WS_KVP + qoff;
            u32 fo = WS_FW + ((rank * 16 + v * 4) * 4 + ((l16 & 3) == 3 ? 0 : l16)) * 128;
            u32 o3 = dt < 4 ? WS_F3 + lane * 4096 + (b >> 2) * 64 + (b & 3) * 8 : OOB;
            asm volatile("" : "+v"(o3));
            asm volatile("" : "+s"(selLo), "+s"(selHi), "+v"(au[0]), "+v"(au[1]), "+v"(ay[0]), "+v"(ay[1]), "+v"(oy), "+v"(ok), "+v"(fo), "+v"(d1), "+v"(d2));
            delay<DLY_F1>();
            { const u32 sp = wait_flags(rw, WS_F1 + ((pair >> 2) * 32 + v * 8 + (lane & 7)) * 128, tag); COUNT(26, sp); }
            STAMP(4);
#pragma unroll
            for (int i = 0; i < 2; ++i) kv[i] = ld4<SC1>(rw, ok + i * 1024, 0);
            const u32 fw = ld1<SC1>(rw, fo, 0);
            SB();
#pragma unroll
            for (int i = 0; i < 2; ++i) {
                lds_st<u32x2>(au[i], u32x2{__builtin_amdgcn_perm(kv[i].z, kv[i].w, 0x05040100u), __builtin_amdgcn_perm(kv[i].x, kv[i].y, 0x05040100u)});
                lds_st<u32x2>(au[i] + UR_CH, u32x2{__builtin_amdgcn_perm(kv[i].z, kv[i].w, 0x07060302u), __builtin_amdgcn_perm(kv[i].x, kv[i].y, 0x07060302u)});
            }
            f32x2 tD[8];
#pragma unroll
            for (int i = 0; i < 8; ++i) { const f32x2 k2 = up2(kv[i >> 2][i & 3]); tD[i] = pfma(k2, d2, k2 * d1); asm volatile("" : "+v"(tD[i])); }
            STAMP(5);
            bar();
            st2<SC1>(u32x2{now(), tag}, rw, o3, 0);
            st4<SC1>(sent4, rw, so, 0);
            COUNT(42, 1);
            if (RARE(__any(fw != tag))) { wait_flags(rw, fo, tag); COUNT(42, 0); }
            fence();
            u32x2 st0 = __builtin_amdgcn_raw_buffer_load_b64(rw, tok * 64, WS_ST0, SC1);
#pragma unroll
            for (int i = 0; i < 6; ++i) cp[i] = ld1<SC1>(rw, WS_CS + (rank * 64 + v * 4 + hh * 2) * 128 + rho * 4 + (i & 1) * 128, (i >> 1) * 2048);
#pragma unroll
            for (int i = 0; i < 24; ++i) R[i] = ld4<SC1>(rw, lane16 + (i & 3) * 1024, WS_WQ + ((rank * 3 + (i >> 3)) * 32 + v * 8 + (i & 4)) * 1024);
            STAMP(34);
            if (CL == 1) clear2();
            if (CL == 1) asm volatile("s_waitcnt vmcnt(35)" ::: "memory"); else asm volatile("s_waitcnt vmcnt(31)" ::: "memory");
            STAMP(14);
            bar();
            STAMP(15);
            u32x2 ya[2], yb[2];
#pragma unroll
            for (int i = 0; i < 2; ++i) { ya[i] = lds_ld<u32x2>(ay[i]); yb[i] = lds_ld<u32x2>(ay[i] + YC_CH); }
#pragma unroll
            for (int i = 0; i < 2; ++i) {
                u32x4 y;
#pragma unroll
                for (int e = 0; e < 4; ++e) {
                    const u32 sel = (e & 1) ? selHi : selLo;
                    y[e] = nos(pk(dot2(ya[i][e >> 1], sel, tD[4 * i + e].x), dot2(yb[i][e >> 1], sel, tD[4 * i + e].y)));
                }
                st4<SC1>(y, rw, oy + i * 1024, 0);
            }
            SB();
            STAMP(23);
            if (RARE(__any(st0.y != tag))) {
                u32 sp = 0;
                do { fence(); st0 = __builtin_amdgcn_raw_buffer_load_b64(rw, tok * 64, WS_ST0, SC1); } while (again(__any(st0.y != tag), sp));
            }
            const float rs = __builtin_amdgcn_rcpf(hi(st0.x)) * 1.0000003f, mean = lo(st0.x);
#pragma unroll
            for (int i = 0; i < 8; ++i) {
                xf[i] = up2(xr[i >> 2][i & 3]);
                const f32x2 n = rb2(rb2(xf[i] - mean) * rs);
                b1[i] = n * 65537.f; b2[i] = n * -65536.f;
                asm volatile("" : "+v"(xf[i]), "+v"(b1[i]), "+v"(b2[i]));
            }
        }
        u32x4 yt[2];
        u32 poff = WS_YP + ((tile * 16 + (lane >> 3)) * 2048 + T * 32 + 4 * (lane & 7)) * 4;
        u32 aWs = L0 + L_W + v * 8192 + lane16;
        asm volatile("" : "+v"(poff), "+v"(aWs));
        auto wo_load = [&](int i0, int i1) __attribute__((always_inline)) {
#pragma unroll
            for (int i = i0; i < i1; ++i) R[i] = ld4<SC1>(rw, lane16 + (i & 3) * 1024, WS_WO + (tile * 32 + (i & ~3)) * 1024);
        };
        STAMP(25);
        bar();
#pragma unroll
        for (int s = 0; s < 2; ++s) yt[s] = ld4<SC1>(rw, poff, s * 65536);
        SB();
        delay<2>();
        if (WO_W2 >= 1) wo_load(24, 32);
        SB();
#pragma unroll
        for (int i = 8; i < 16; ++i) lds_st<u32x4>(aWs + (33 + (i & 7)) * 1024, R[i]);
        SB();
        if (WO_W2 >= 2) wo_load(8, 16);
        if (CL == 3) clear2();
        SB();
        COUNT(41, 0);
        if (RARE(__any(max4(max4(0, yt[0]), yt[1]) == SENT))) {
            u32 sp = 0;
            do { fence(); yt[0] = ld4<SC1>(rw, poff, 0); yt[1] = ld4<SC1>(rw, poff, 65536); } while (again(__any(max4(max4(0, yt[0]), yt[1]) == SENT), sp));
            COUNT(41, sp + 1);
        }
        STAMP(13);
#pragma unroll
        for (int s = 0; s < 2; ++s) {
            yt[s] = t1(yt[s]);
#pragma unroll
            for (int i = 0; i < 4; ++i) { const f32x2 y = up2(yt[s][i]); xr[s][i] = nos(pk2(xf[4 * s + i] + pfma(y, b2[4 * s + i], y * b1[4 * s + i]))); }
            st4<SC1>(xr[s], rw, xoff + s * 1024, 0);
        }
        SB();
        STAMP(3);
        u32 tb4;
        auto at_b4 = [&](u32 layer) __attribute__((always_inline)) {
            bar();
            stats_publish(xr, rw, dpos, dslot + 2 * layer * DS_BUF);
            tb4 = now();
            if (TS_PACE) st4<SC1>(u32x4{tb4, tag, tb4, tag}, rw, dt < 8 ? WS_TS + ((layer * 16 + rank) * 16 + grp) * 128 + lane16 : OOB, 0);
            SB();
        };
        u32x2 ts;
        auto ln_to_lds = [&](u32 layer, auto meanwhile) __attribute__((always_inline)) {
            const u32 off = WS_DS + 2 * layer * DS_BUF + (grp * 4 + v) * 4096;
            u32x4 ds[4]; float t, mean, rs;
            meanwhile();
            SB();
            wait_until(tb4, SC_WAIT);
            stats_issue(ds, rw, lane16, off);
            STAMP(45 + layer);
            bar();
            const u32 sp = stats_check(ds, rw, lane16, off, t);
            COUNT(21, sp);
            stats_finish(t, mean, rs);
            const u32 nm = pk(-mean, -mean);
            lds_st<u32x4>(L0 + L_LM + (v * 32 + rho) * 16, u32x4{nm, nm, nm, nm});
            lds_st<f32x2>(L0 + L_LR + (v * 32 + rho) * 8, f32x2{rs, rs * 1.44269504088896f});
            STAMP(7 + 10 * layer);
        };
        at_b4(0);
        aWs += 66 * 1024;
        asm volatile("" : "+v"(aWs));
#pragma unroll
        for (int i = 16; i < 24; ++i) lds_st<u32x4>(aWs + (i & 7) * 1024, R[i]);
        ln_to_lds(0, [&]() __attribute__((always_inline)) {
#pragma unroll
            for (int i = 0; i < 8; ++i) lds_st<u32x4>(L0 + L_W + v * 8192 + lane16 + i * 1024, R[i]);
#pragma unroll
            for (int p = 0; p < 3; ++p) {
                const float cs = halfsum(fasu(cp[2 * p]) + fasu(cp[2 * p + 1]));
                const float c0 = rb(cs), c1 = rb(cs - c0), c2 = cs - c0 - c1;
                lds_st<u32x2>(L0 + L_W + (p * 33 + 32) * 1024 + ((v >> 1) * 32 + rho) * 16 + (v & 1) * 8, u32x2{pk(c0, c1), pk(c2, 0.f)});
            }
            if (dt < 5) lds_st<u32x4>(dt < 4 ? L0 + L_ZERO + lane16 : L0 + L_OK, u32x4{0, 0, 0, 0});
        });
        const u32 head = v >> 1, fh = v & 1;
        u32 aO = L0 + L_O + lane16;
        u32 aP = L0 + L_IMG + (4 * g16 + (l16 >> 2)) * IMG_RS + (l16 & 3) * 8 + head * 64 + fh * 32, aV_ = aP + 128 - head * 32 - fh * 32;
        u32 spw = WS_SP + (rank * 16 + grp) * SP_UNIT + v * SP_WAVE + l16 * 32 + g16 * 8, spz = l16 == 0 ? spw + 512 + g16 * 8 : OOB;
        u32 spr = WS_SP + rank * 16 * SP_UNIT + (g16 >> 1) * SP_UNIT + v * SP_WAVE + l16 * 32 + (g16 & 1) * 16;
        u32 spy = WS_SP + rank * 16 * SP_UNIT + g16 * SP_UNIT + v * SP_WAVE + 512 + l16 * 4;
        u32 aK_ = L0 + L_KVT + (head * 16 + l16) * KVT_RS + fh * 32 + g16 * 8, aZ_ = L0 + L_Z + head * 64 + fh * 32 + l16 * 2;
        u32x4 idm;
        {
            const u32 e = l16 & 7, one = (l16 >> 3) == (g16 & 1) ? ((e & 1) ? ONE_HI : ONE_LO) : 0u;
            idm = u32x4{(e >> 1) == 0 ? one : 0u, (e >> 1) == 1 ? one : 0u, (e >> 1) == 2 ? one : 0u, (e >> 1) == 3 ? one : 0u};
        }
        asm volatile("" : "+v"(aO), "+v"(aP), "+v"(aV_), "+v"(spw), "+v"(spz), "+v"(spr), "+v"(spy), "+v"(aK_), "+v"(aZ_), "+v"(idm));
        auto at_bs = [&](u32 layer) __attribute__((always_inline)) {
            bar();
            if (TS_PACE) ts = __builtin_amdgcn_raw_buffer_load_b64(rw, WS_TS + ((layer * 16 + rank) * 16 + l16) * 128, 0, SC1);
        };
        at_bs(0);
        if (CL == 2) clear2();
        wo_load(0, 8);
        if (WO_W2 < 2) wo_load(8, 16);
        wo_load(16, 24);
        if (WO_W2 < 1) wo_load(24, 32);
        if (CL == 2) asm volatile("s_waitcnt vmcnt(%c0)" ::"i"(32 - 8 * WO_W2) : "memory");
        STAMP(35);
#pragma unroll 1
        for (int layer = 0; layer < 2; ++layer) {
            u32x4 ones = {ONE2, ONE2, ONE2, ONE2};
            asm volatile("" : "+v"(aO), "+v"(aP), "+v"(aV_), "+v"(spw), "+v"(spz), "+v"(spr), "+v"(spy), "+v"(aK_), "+v"(aZ_), "+v"(idm), "+v"(ones));
            bar();
            STAMP(6 + 10 * layer);
            {
                u32x2 p[8], q[8];
#pragma unroll
                for (int ks = 0; ks < 4; ++ks) {
                    p[2 * ks] = lds_tr(aP + ks * 32 * IMG_RS); p[2 * ks + 1] = lds_tr(aP + (ks * 32 + 16) * IMG_RS);
                    q[2 * ks] = lds_tr(aV_ + ks * 32 * IMG_RS); q[2 * ks + 1] = lds_tr(aV_ + (ks * 32 + 16) * IMG_RS);
                }
                SB();
                f32x4 kvp = {}, zp = {};
#pragma unroll
                for (int ks = 0; ks < 4; ++ks) {
                    const u32x4 pa = {p[2 * ks].x, p[2 * ks].y, p[2 * ks + 1].x, p[2 * ks + 1].y};
                    kvp = mfma16(pa, u32x4{q[2 * ks].x, q[2 * ks].y, q[2 * ks + 1].x, q[2 * ks + 1].y}, kvp);
                    zp = mfma16(pa, ones, zp);
                }
                st2<SC1>(u32x2{nos(pk(kvp[0], kvp[1])), nos(pk(kvp[2], kvp[3]))}, rw, spw, 0);
                st4<SC1>(nos4(__builtin_bit_cast(u32x4, zp)), rw, spz, 0);
            }
            SB();
            const u32 tsp = now();
            STAMP(layer ? 39 : 38);
            int lag = 0;
            if (TS_PACE) {
                if (RARE(__any(ts.y != tag))) {
                    u32 sp = 0;
                    do { fence(); ts = __builtin_amdgcn_raw_buffer_load_b64(rw, WS_TS + ((layer * 16 + rank) * 16 + l16) * 128, 0, SC1); } while (again(__any(ts.y != tag), sp));
                }
                lag = min(__builtin_amdgcn_readfirstlane(rowmax((int)(ts.x - tb4))), 1000);
            }
            COUNT(43 + layer, lag);
            wait_until(tsp, lag + ST_WAIT);
            {
                u32 sp = 0; bool bad; f32x4 t; float z;
                do {
                    asm volatile("" : "+v"(spr), "+v"(spy) :: "memory");
                    u32x4 pt[8]; u32 zt[4];
#pragma unroll
                    for (int i = 0; i < 4; ++i) zt[i] = ld1<SC1>(rw, spy, 4 * i * SP_UNIT);
#pragma unroll
                    for (int i = 0; i < 8; ++i) pt[i] = ld4<SC1>(rw, spr, 2 * i * SP_UNIT);
                    SB();
                    float za = fasu(zt[0]) + fasu(zt[1]), zb = fasu(zt[2]) + fasu(zt[3]);
                    asm volatile("" : "+v"(za), "+v"(zb));
                    z = halfsum(rowpairsum(za + zb));
                    lds_st<u16>(aZ_, (u16)pk(z, z));
                    SB();
                    t = f32x4{};
#pragma unroll
                    for (int i = 0; i < 8; ++i) t = mfma16(idm, pt[i], t);
                    lds_st<u32x2>(aK_, u32x2{pk(t[0], t[1]), pk(t[2], t[3])});
                    SB();
                    const float chk = t[0] + z;
                    bad = __any(chk != chk);
                    if (RARE(bad)) {
                        u32 m = max(max(zt[0], zt[1]), max(zt[2], zt[3]));
#pragma unroll
                        for (int i = 0; i < 8; ++i) m = max4(m, pt[i]);
                        bad = __any(m == SENT);
                    }
                } while (again(bad, sp));
                COUNT(28 + layer, sp);
            }
            STAMP(8 + 10 * layer);
            bar();
            f32x16 acc, res;
#pragma unroll
            for (int i = 0; i < 8; ++i) { res[2 * i] = lo(xr[i >> 2][i & 3]); res[2 * i + 1] = hi(xr[i >> 2][i & 3]); }
            asm volatile("" : "+v"(res));
            u32 spo = 0;
            if (!O_DMA) {
                bar();
                delay<DLY_O>();
                spo = o_gather(rw, lane16, WS_OF + ((layer * 64 + T) * 32 + wave * 4) * 1024, aO + wave * 4096);
            }
            bar();
            STAMP(9 + 10 * layer);
            {
                u32 sp = 0; bool bad;
                do {
                    u32x4 of[4];
#pragma unroll
                    for (int i = 0; i < 3; ++i) of[i] = lds_ld<u32x4>(aO + i * 1024);
                    SB();
#pragma unroll
                    for (int i = 0; i < 32; ++i) {
                        if (i + 3 < 32) of[(i + 3) & 3] = lds_ld<u32x4>(aO + (i + 3) * 1024);
                        acc = mfma32(R[i], of[i & 3], i == 0 ? res : acc);
                        SB();
                    }
                    bad = O_DMA && __any(acc[0] != acc[0]);
                    if (RARE(bad)) {
                        bad = sp == 0;
                        sp = 1;
                        u32 sq = 0;
                        do { fence(); } while (again(lds_ld<u32>(L0 + L_OK) < 4u * (layer + 1), sq));
                        fence();
                    }
                } while (RARE(bad));
                COUNT(30 + layer, sp + spo);
            }
#pragma unroll
            for (int i = 0; i < 8; ++i) xr[i >> 2][i & 3] = pk(acc[2 * i], acc[2 * i + 1]);
            STAMP(10 + 10 * layer);
            if (layer == 1) {
                const rsrc_t ro = mkrs(gout, S * D * 2);
                const u32 roff = (tok * D + tile * 32 + hh * 8) * 2;
                st4<0>(xr[0], ro, roff, 0);
                st4<0>(xr[1], ro, roff + 32, 0);
                STAMP(22);
                break;
            }
            stats_publish(xr, rw, dpos, dslot + DS_BUF);
            SB();
            asm volatile("" : "+v"(yt[0]), "+v"(yt[1]));
#pragma unroll
            for (int i = 0; i < 8; ++i) {
                xf[i] = up2(xr[i >> 2][i & 3]);
                const f32x2 y = up2(yt[i >> 2][i & 3]);
                b1[i] = y * 65537.f; b2[i] = y * -65536.f;
                asm volatile("" : "+v"(xf[i]), "+v"(b1[i]), "+v"(b2[i]));
            }
            spw += 256 * SP_UNIT; spz += 256 * SP_UNIT; spr += 256 * SP_UNIT; spy += 256 * SP_UNIT;
            delay<DLY_DS>();
            float mean, rs;
            { u32x4 ds[4]; float t; stats_issue(ds, rw, lane16, WS_DS + DS_BUF + T * 4096); const u32 sp = stats_check(ds, rw, lane16, WS_DS + DS_BUF + T * 4096, t); COUNT(24, sp); stats_finish(t, mean, rs); }
            STAMP(11);
#pragma unroll
            for (int s = 0; s < 2; ++s) {
#pragma unroll
                for (int i = 0; i < 4; ++i) {
                    const f32x2 n = rb2(rb2(xf[4 * s + i] - mean) * rs);
                    xr[s][i] = nos(pk2(xf[4 * s + i] + pfma(n, b2[4 * s + i], n * b1[4 * s + i])));
                }
                st4<SC1>(xr[s], rw, xoff + MB2 + s * 1024, 0);
            }
            SB();
            STAMP(12);
            at_b4(1);
            ln_to_lds(1, []() {});
            at_bs(1);
        }
        st1<0>(tag, rw, b == 0 && tid == 256 ? WS_E : OOB, 0);
        DUMP();
        __builtin_amdgcn_endpgm();
    }
}

extern "C" void kernel_launch(void* const* d_in, const int* in_sizes, int n_in, void* d_out, int out_size, void* d_ws, size_t ws_size, hipStream_t stream) {
    (void)hipFuncSetAttribute((const void*)h3_hedgehog_kernel, hipFuncAttributeMaxDynamicSharedMemorySize, LDS_BYTES);
    h3_hedgehog_kernel<<<256, 512, LDS_BYTES, stream>>>((const u16*)d_in[0], (const u16*)d_in[1], (const u16*)d_in[2], (const u16*)d_in[3], (const u16*)d_in[4],
                                                        (const u16*)d_in[5], (const u16*)d_in[7], (const u16*)d_in[8], (const u16*)d_in[9], (u16*)d_out, (char*)d_ws);
}
```
